```python
import torch

def _softmax(x, axis=-1):
    return torch.softmax(x, dim=axis)
def _gelu(x):
    return torch.nn.functional.gelu(x, approximate="tanh")
def _silu(x):
    return torch.nn.functional.silu(x)
def _sigmoid(x):
    return torch.sigmoid(x)
def _layernorm(x, axis=-1, eps=1e-5):
    m = x.mean(dim=axis, keepdim=True)
    v = x.var(dim=axis, keepdim=True, unbiased=False)
    return (x - m) / torch.sqrt(v + eps)
def _scatter_add(tgt, idx, src, axis=0):
    return tgt.clone().index_add_(axis, idx, src)
def _segment_sum(src, seg, n):
    out = torch.zeros((n,) + src.shape[1:], dtype=src.dtype)
    return out.index_add_(0, seg, src)
def _pad(x, pw):
    flat = tuple(v for pair in reversed(pw) for v in pair)
    return torch.nn.functional.pad(x, flat)

kernel_name = 'gated_mix__grouped_kv_mqa__geglu_ffn__prompt'

def setup_inputs():

    _w = (lambda a: (lambda t: t.to(torch.bfloat16) if t.is_floating_point() else t)(torch.as_tensor(a)))
    def _A_setup():
        B, S, D, H = 1, 2048, 640, 32
        Dh = D // H
        return {
            "x": _w((lambda *s: torch.randn(*s, dtype=torch.bfloat16) * 0.05)(B, S, D)),
            "wq": _w((lambda *s: torch.randn(*s, dtype=torch.bfloat16) * 0.8)(D, H * Dh)),
            "wk": _w((lambda *s: torch.randn(*s, dtype=torch.bfloat16) * 0.8)(D, Dh)),
            "wv": _w((lambda *s: torch.randn(*s, dtype=torch.bfloat16) * 0.05)(D, Dh)),
            "wo": _w((lambda *s: torch.randn(*s, dtype=torch.bfloat16) * 0.05)(H * Dh, D)),
        }
    _A_in = _A_setup()

    def _B_setup():
        B, S, D, F = 1, 2048, 640, 1728
        return {
            "x": _w((lambda *s: torch.randn(*s, dtype=torch.bfloat16) * 0.05)(B, S, D)),
            "w_gate": _w((lambda *s: torch.randn(*s, dtype=torch.bfloat16) * 0.05)(D, F)),
            "w_up": _w((lambda *s: torch.randn(*s, dtype=torch.bfloat16) * 0.05)(D, F)),
            "w_down": _w((lambda *s: torch.randn(*s, dtype=torch.bfloat16) * 0.05)(F, D)),
        }
    _B_in = _B_setup()

    return {
        "A_x": _A_in["x"],
        "A_wq": _A_in["wq"],
        "A_wk": _A_in["wk"],
        "A_wv": _A_in["wv"],
        "A_wo": _A_in["wo"],
        "B_x": _B_in["x"],
        "B_w_gate": _B_in["w_gate"],
        "B_w_up": _B_in["w_up"],
        "B_w_down": _B_in["w_down"],
        "gate_w": _w((lambda *s: torch.randn(*s, dtype=torch.bfloat16) * 0.05)(640) * 16.0),
    }

def reference(A_x, A_wq, A_wk, A_wv, A_wo, B_x, B_w_gate, B_w_up, B_w_down, gate_w):
    def _A_ref(x, wq, wk, wv, wo):
        B, S, D = x.shape
        H = 32
        Dh = D // H
        q = (lambda x, *d: x.permute(*d))((x @ wq).reshape(B, S, H, Dh), 0, 2, 1, 3)
        k = x @ wk
        v = x @ wv
        scores = torch.einsum("bhqd,bkd->bhqk", q, k) * (Dh ** -0.5)
        p = _softmax(scores, axis=-1)
        o = torch.einsum("bhqk,bkd->bhqd", p, v)
        return (lambda x, *d: x.permute(*d))(o, 0, 2, 1, 3).reshape(B, S, H * Dh) @ wo

    def _B_ref(x, w_gate, w_up, w_down):
        return (_gelu(x @ w_gate) * (x @ w_up)) @ w_down

    x = A_x
    g = _sigmoid((x * gate_w).sum(-1, keepdims=True))
    a = _A_ref(x, A_wq, A_wk, A_wv, A_wo)
    b = _B_ref(x, B_w_gate, B_w_up, B_w_down)
    return g * a + (1 - g) * b
```

```cpp
#include <hip/hip_runtime.h>
#include <hip/hip_bf16.h>
#include <stdint.h>

typedef __bf16 bf16_t;
typedef __bf16 bf16x2 __attribute__((ext_vector_type(2)));
typedef __bf16 bf16x4 __attribute__((ext_vector_type(4)));
typedef __bf16 bf16x8 __attribute__((ext_vector_type(8)));
typedef __bf16 bf16x16 __attribute__((ext_vector_type(16)));
typedef short s16x4 __attribute__((ext_vector_type(4)));
typedef float f32x2 __attribute__((ext_vector_type(2)));
typedef float f32x4 __attribute__((ext_vector_type(4)));
typedef float f32x8 __attribute__((ext_vector_type(8)));
typedef float f32x16 __attribute__((ext_vector_type(16)));
typedef unsigned u32x2 __attribute__((ext_vector_type(2)));
typedef unsigned u32x4 __attribute__((ext_vector_type(4)));
#define LDSP __attribute__((address_space(3)))
#define DI __device__ __forceinline__

constexpr int S = 2048, D = 640, H = 32, DH = 20, F = 1728;
constexpr float QSCALE = 0.22360679774997896f * 1.4426950408889634f;

constexpr size_t WS_Q = 0;
constexpr size_t WS_O = WS_Q + (size_t)S * D * 2;
constexpr size_t WS_H = WS_O + (size_t)S * D * 2;
constexpr int KV_CHUNK = 21504;
constexpr size_t WS_KV = WS_H + (size_t)S * F * 2;
constexpr size_t WS_G = WS_KV + 8 * KV_CHUNK;
constexpr size_t WS_KM = WS_G + (size_t)S * 4;

DI unsigned pk2(float lo, float hi) { f32x2 v = {lo, hi}; bf16x2 b = __builtin_convertvector(v, bf16x2); return __builtin_bit_cast(unsigned, b); }
DI unsigned rfl(unsigned x) { return __builtin_amdgcn_readfirstlane(x); }
DI void dma16(const void* sbase, unsigned voff, unsigned ldsaddr) {
  asm volatile("s_mov_b32 m0, %2\n\ts_nop 0\n\tglobal_load_lds_dwordx4 %0, %1" :: "v"(voff), "s"(sbase), "s"(ldsaddr) : "memory");
}
#define WAITV(n) asm volatile("s_waitcnt vmcnt(" #n ")" ::: "memory")
#define BARRIER() do { asm volatile("s_waitcnt lgkmcnt(0)" ::: "memory"); __builtin_amdgcn_s_barrier(); asm volatile("" ::: "memory"); } while (0)
DI bf16x8 lds_b128(unsigned a) { return *(LDSP const bf16x8*)(size_t)a; }
DI bf16x4 lds_b64(unsigned a) { return *(LDSP const bf16x4*)(size_t)a; }
DI bf16x4 lds_tr(unsigned a) { return __builtin_amdgcn_ds_read_tr16_b64_v4bf16((LDSP bf16x4*)(size_t)a); }
DI bf16x8 cat4(bf16x4 a, bf16x4 b) { return __builtin_shufflevector(a, b, 0, 1, 2, 3, 4, 5, 6, 7); }
DI f32x16 mfma32(bf16x8 a, bf16x8 b, f32x16 c) { return __builtin_amdgcn_mfma_f32_32x32x16_bf16(a, b, c, 0, 0, 0); }

constexpr int K1_X = 0, K1_WL = 32768, K1_WH = 40960, K1_EX = 49152, K1_STAGE = 52224, K1_GW = 3 * K1_STAGE, K1_LDS = K1_GW + 2048;
enum { T_FFN = 0, T_Q = 1, T_QK = 2, T_QV = 3 };

template <int TYPE>
DI void k1_body(const bf16_t* __restrict__ x, const bf16_t* __restrict__ wlo, const bf16_t* __restrict__ whi, int ldw, const bf16_t* __restrict__ wex,
                const bf16_t* __restrict__ gate_w, int rb, int col0, char* __restrict__ ws) {
  const unsigned tid = threadIdx.x, lane = tid & 63, wid = rfl(tid >> 6);
  const unsigned wm = wid >> 1, wn = wid & 1;
  const unsigned l31 = lane & 31, hh = lane >> 5;
  unsigned vx[4];
#pragma unroll
  for (int i = 0; i < 4; ++i) { unsigned row = (wid * 4 + i) * 8 + (lane >> 3), slot = lane & 7; vx[i] = row * (D * 2) + ((slot ^ ((row >> 1) & 7)) << 4); }
  unsigned vw;
  { unsigned kr = wid * 8 + (lane >> 3), slot = lane & 7, b = (slot >> 2) ^ ((kr >> 1) & 1); vw = kr * (ldw * 2) + (b * 4 + (slot & 3)) * 16; }
  unsigned vex = (wid * 64 + lane) * 16; if (vex >= 2560) vex -= 512;
  const char* xb = (const char*)(x + (size_t)rb * 256 * D);
  auto stage = [&](int s, int slot) {
    const unsigned base = slot * K1_STAGE;
    if (TYPE >= T_QK) { if (wid < 3) dma16((const char*)wex + s * 2560, vex, base + K1_EX + wid * 1024); }
#pragma unroll
    for (int i = 0; i < 4; ++i) dma16(xb + s * 128, vx[i], base + K1_X + (wid * 4 + i) * 1024);
    dma16((const char*)wlo + (size_t)s * 64 * ldw * 2, vw, base + K1_WL + wid * 1024);
    dma16((const char*)whi + (size_t)s * 64 * ldw * 2, vw, base + K1_WH + wid * 1024);
  };
  if (TYPE == T_QK) { if (wid < 2) { unsigned o = (wid * 64 + lane) * 16; if (o >= 1280) o = 0; dma16(gate_w, o, K1_GW + wid * 1024); } }
  stage(0, 0); stage(1, 1);
  unsigned ax[4];
#pragma unroll
  for (int kk = 0; kk < 4; ++kk) ax[kk] = K1_X + (wm * 64 + l31) * 128 + (((2 * kk + hh) ^ ((l31 >> 1) & 7)) << 4);
  const unsigned q4 = (lane & 15) >> 2, p4 = lane & 3, b16 = (lane >> 4) & 1;
  const unsigned aw = K1_WL + (8 * hh + q4) * 128 + ((wn ^ ((q4 >> 1) & 1)) * 64) + 32 * b16 + 8 * p4;
  const unsigned aex = K1_EX + (8 * hh + q4) * 40 + 32 * b16 + 8 * p4;
  const unsigned agw = K1_GW + 16 * hh;
  f32x16 acc[2][2] = {}, accx = {};
  for (int s = 0; s < 10; ++s) {
    const int slot = s % 3;
    if (s + 1 < 10) WAITV(6); else WAITV(0);
    BARRIER();
    if (s + 2 < 10) stage(s + 2, (s + 2) % 3);
    const unsigned sb = slot * K1_STAGE;
#pragma unroll
    for (int kk = 0; kk < 4; ++kk) {
      bf16x8 xf0 = lds_b128(sb + ax[kk]), xf1 = lds_b128(sb + ax[kk] + 4096);
      bf16x8 wl = cat4(lds_tr(sb + aw + kk * 2048), lds_tr(sb + aw + kk * 2048 + 512));
      bf16x8 wh = cat4(lds_tr(sb + aw + 8192 + kk * 2048), lds_tr(sb + aw + 8192 + kk * 2048 + 512));
      acc[0][0] = mfma32(wl, xf0, acc[0][0]);
      acc[0][1] = mfma32(wl, xf1, acc[0][1]);
      acc[1][0] = mfma32(wh, xf0, acc[1][0]);
      acc[1][1] = mfma32(wh, xf1, acc[1][1]);
      if (TYPE >= T_QK) {
        bf16x8 we = cat4(lds_tr(sb + aex + kk * 640), lds_tr(sb + aex + kk * 640 + 160));
        bf16x8 xe = wn ? xf1 : xf0;
        if (TYPE == T_QK) {
          bf16x8 gw = lds_b128(agw + s * 128 + kk * 32);
          if (l31 == 20) we = gw;
          accx = mfma32(we, xe, accx);
        } else {
          accx = mfma32(xe, we, accx);
        }
      }
    }
  }
  const int row0 = rb * 256 + wm * 64;
  if (TYPE == T_FFN) {
    bf16_t* h = (bf16_t*)(ws + WS_H);
    constexpr float C0 = -2.0f * 1.4426950408889634f * 0.7978845608028654f, C1 = C0 * 0.044715f;
#pragma unroll
    for (int j = 0; j < 2; ++j) {
      unsigned pk[8];
#pragma unroll
      for (int r = 0; r < 16; r += 2) {
        float v[2];
#pragma unroll
        for (int e = 0; e < 2; ++e) {
          float gt = acc[0][j][r + e], up = acc[1][j][r + e];
          float z = gt * __builtin_fmaf(gt * gt, C1, C0);
          float sg = __builtin_amdgcn_rcpf(1.0f + __builtin_amdgcn_exp2f(z));
          v[e] = gt * sg * up;
        }
        pk[r >> 1] = pk2(v[0], v[1]);
      }
#pragma unroll
      for (int g = 0; g < 4; g += 2) {
        auto r0 = __builtin_amdgcn_permlane32_swap(pk[2 * g], pk[2 * g + 2], false, false);
        auto r1 = __builtin_amdgcn_permlane32_swap(pk[2 * g + 1], pk[2 * g + 3], false, false);
        u32x4 o = {r0[0], r1[0], r0[1], r1[1]};
        bf16_t* dst = h + (size_t)(row0 + j * 32 + l31) * F + col0 + wn * 32 + g * 8 + hh * 8;
        *(u32x4*)dst = o;
      }
    }
  } else {
    bf16_t* q = (bf16_t*)(ws + WS_Q);
#pragma unroll
    for (int n = 0; n < 2; ++n)
#pragma unroll
      for (int j = 0; j < 2; ++j) {
        unsigned pk[8];
#pragma unroll
        for (int r = 0; r < 16; r += 2) pk[r >> 1] = pk2(acc[n][j][r] * QSCALE, acc[n][j][r + 1] * QSCALE);
#pragma unroll
        for (int g = 0; g < 4; g += 2) {
          auto r0 = __builtin_amdgcn_permlane32_swap(pk[2 * g], pk[2 * g + 2], false, false);
          auto r1 = __builtin_amdgcn_permlane32_swap(pk[2 * g + 1], pk[2 * g + 3], false, false);
          u32x4 o = {r0[0], r1[0], r0[1], r1[1]};
          bf16_t* dst = q + (size_t)(row0 + j * 32 + l31) * D + col0 + n * 64 + wn * 32 + g * 8 + hh * 8;
          *(u32x4*)dst = o;
        }
      }
    const int key0 = row0 + wn * 32;
    char* kv = ws + WS_KV + (size_t)(key0 >> 8) * KV_CHUNK;
    const int t32 = (key0 >> 5) & 7;
    if (TYPE == T_QK) {
      unsigned pk[6];
#pragma unroll
      for (int r = 0; r < 12; r += 2) pk[r >> 1] = pk2(accx[r], accx[r + 1]);
      *(u32x2*)(kv + t32 * 1024 + l31 * 16 + hh * 8) = u32x2{pk[0], pk[1]};
      *(u32x2*)(kv + t32 * 1024 + 512 + l31 * 16 + hh * 8) = u32x2{pk[2], pk[3]};
      if (hh == 0) *(u32x2*)(kv + 8192 + t32 * 256 + l31 * 8) = u32x2{pk[4], pk[5]};
      else ((float*)(ws + WS_G))[key0 + l31] = 1.0f / (1.0f + __expf(-accx[8]));
      float n2 = 0.f;
#pragma unroll
      for (int i = 0; i < 6; ++i) {
        if (i < 4 || hh == 0) { float a = __uint_as_float(pk[i] << 16), b = __uint_as_float(pk[i] & 0xffff0000u); n2 += a * a + b * b; }
      }
      n2 += __shfl_xor(n2, 32);
#pragma unroll
      for (int o = 16; o >= 1; o >>= 1) n2 = fmaxf(n2, __shfl_xor(n2, o));
      if (lane == 0) ((float*)(ws + WS_KM))[key0 >> 5] = n2;
    } else if (TYPE == T_QV) {
      if (l31 <= 20) {
#pragma unroll
        for (int g = 0; g < 4; ++g) {
          u32x2 v = {pk2(accx[4 * g], accx[4 * g + 1]), pk2(accx[4 * g + 2], accx[4 * g + 3])};
          if (l31 == 20) v = u32x2{0x3f803f80u, 0x3f803f80u};
          *(u32x2*)(kv + 10240 + (t32 * 2 + (g >> 1)) * 672 + (g & 1) * 336 + l31 * 16 + hh * 8) = v;
        }
      }
    }
  }
}

__global__ __launch_bounds__(512, 2) void k1_proj(const bf16_t* __restrict__ x, const bf16_t* __restrict__ wq, const bf16_t* __restrict__ wk, const bf16_t* __restrict__ wv,
                                                  const bf16_t* __restrict__ wg, const bf16_t* __restrict__ wu, const bf16_t* __restrict__ gate_w, char* __restrict__ ws) {
  const int b = blockIdx.x, xcd = b & 7, idx = b >> 3;
  const int rb = (xcd & 1) * 4 + (idx & 3), cb = (xcd >> 1) * 8 + (idx >> 2);
  if (cb < 27) k1_body<T_FFN>(x, wg + cb * 64, wu + cb * 64, F, nullptr, nullptr, rb, cb * 64, ws);
  else {
    const int c = cb - 27;
    if (c == 0) k1_body<T_QK>(x, wq + c * 128, wq + c * 128 + 64, D, wk, gate_w, rb, c * 128, ws);
    else if (c == 1) k1_body<T_QV>(x, wq + c * 128, wq + c * 128 + 64, D, wv, nullptr, rb, c * 128, ws);
    else k1_body<T_Q>(x, wq + c * 128, wq + c * 128 + 64, D, nullptr, nullptr, rb, c * 128, ws);
  }
}

constexpr int K2_SLOT = KV_CHUNK, K2_LDS = 2 * 3 * K2_SLOT;
DI f32x16 mfma8(bf16x4 a, bf16x4 b, f32x16 c) { return __builtin_amdgcn_mfma_f32_32x32x8bf16_1k(__builtin_bit_cast(s16x4, a), __builtin_bit_cast(s16x4, b), c, 0, 0, 0); }
DI f32x16 pv_lo(bf16x8 v, f32x16 p, f32x16 o) {
  f32x8 h = __builtin_shufflevector(p, p, 0, 1, 2, 3, 4, 5, 6, 7);
  return __builtin_amdgcn_smfmac_f32_32x32x32_bf16(v, __builtin_bit_cast(bf16x16, h), o, (int)0xDDDDDDDD, 0, 0);
}
DI f32x16 pv_hi(bf16x8 v, f32x16 p, f32x16 o) {
  f32x8 h = __builtin_shufflevector(p, p, 8, 9, 10, 11, 12, 13, 14, 15);
  return __builtin_amdgcn_smfmac_f32_32x32x32_bf16(v, __builtin_bit_cast(bf16x16, h), o, (int)0xDDDDDDDD, 0, 0);
}
#define SB() __builtin_amdgcn_sched_barrier(0)
#define EXP4(s, r) do { s[r] = __builtin_amdgcn_exp2f(s[r]); s[r + 1] = __builtin_amdgcn_exp2f(s[r + 1]); s[r + 2] = __builtin_amdgcn_exp2f(s[r + 2]); s[r + 3] = __builtin_amdgcn_exp2f(s[r + 3]); } while (0)

__global__ __launch_bounds__(512, 2) void k2_attn(char* __restrict__ ws) {
  const unsigned tid = threadIdx.x, lane = tid & 63, wid = rfl(tid >> 6);
  const unsigned rg = wid & 3, kh = wid >> 2, l31 = lane & 31, hh = lane >> 5;
  const int b = blockIdx.x;
  const int qb = b >> 3, hg = b & 7;
  const int head = hg * 4 + rg, q0 = qb * 64;
  const char* kvg = ws + WS_KV + (size_t)kh * 4 * KV_CHUNK;
  const unsigned ldsh = kh * 3 * K2_SLOT;
  auto stage = [&](int c) {
    const unsigned slot = c % 3;
#pragma unroll
    for (int i = 0; i < 6; ++i) { const unsigned p = rg + 4 * i; if (p < 21) dma16(kvg + (size_t)c * KV_CHUNK + p * 1024, lane * 16, ldsh + slot * K2_SLOT + p * 1024); }
  };
  stage(0); stage(1);
  const bf16_t* q = (const bf16_t*)(ws + WS_Q);
  bf16x8 qa[2]; bf16x4 qb4[2];
#pragma unroll
  for (int j = 0; j < 2; ++j) {
    const bf16_t* p = q + (size_t)(q0 + j * 32 + l31) * D + head * DH;
    qa[j] = *(const bf16x8*)(p + 8 * hh);
    bf16x4 t = *(const bf16x4*)(p + 16);
    qb4[j] = hh ? bf16x4{} : t;
  }
  float km = ((const float*)(ws + WS_KM))[lane];
#pragma unroll
  for (int o = 32; o >= 1; o >>= 1) km = fmaxf(km, __shfl_xor(km, o));
  float cb[2];
  bool unsafe = false;
#pragma unroll
  for (int j = 0; j < 2; ++j) {
    float n2 = 0.f;
#pragma unroll
    for (int i = 0; i < 8; ++i) { float a = (float)qa[j][i]; n2 += a * a; }
#pragma unroll
    for (int i = 0; i < 4; ++i) { float a = (float)qb4[j][i]; n2 += a * a; }
    n2 += __shfl_xor(n2, 32);
    cb[j] = sqrtf(n2 * km) * 1.0001f;
    unsafe |= !(cb[j] <= 50.0f);
  }
  if (__any(unsafe)) {
    const char* kall = ws + WS_KV;
    float mx[2] = {-INFINITY, -INFINITY};
    for (int t = 0; t < 64; ++t) {
      const char* kc = kall + (size_t)(t >> 3) * KV_CHUNK;
      bf16x8 ka = *(const bf16x8*)(kc + (t & 7) * 1024 + lane * 16);
      bf16x4 kb = *(const bf16x4*)(kc + 8192 + (t & 7) * 256 + l31 * 8);
#pragma unroll
      for (int j = 0; j < 2; ++j) {
        f32x16 s = mfma32(ka, qa[j], f32x16{});
        s = mfma8(kb, qb4[j], s);
#pragma unroll
        for (int r = 0; r < 16; ++r) mx[j] = fmaxf(mx[j], s[r]);
      }
    }
#pragma unroll
    for (int j = 0; j < 2; ++j) cb[j] = fmaxf(mx[j], __shfl_xor(mx[j], 32));
  }
  f32x16 negm[2];
#pragma unroll
  for (int j = 0; j < 2; ++j)
#pragma unroll
    for (int r = 0; r < 16; ++r) negm[j][r] = -cb[j];

  const unsigned aK = lane * 16, aKB = 8192 + l31 * 8, aV = 10240 + hh * 336 + l31 * 16;
  f32x16 o0 = {}, o1 = {};
  f32x16 sa0, sa1, sb0, sb1;
  WAITV(0); BARRIER();
  stage(2);
  unsigned cur = ldsh;
  bf16x8 kaN; bf16x4 kbN;
  bf16x8 v0, v1;
  {
    bf16x8 ka = lds_b128(cur + aK); bf16x4 kb = lds_b64(cur + aKB);
    kaN = lds_b128(cur + aK + 1024); kbN = lds_b64(cur + aKB + 256);
    v0 = lds_b128(cur + aV); v1 = lds_b128(cur + aV + 672);
    sa0 = mfma32(ka, qa[0], negm[0]); sa0 = mfma8(kb, qb4[0], sa0);
    sa1 = mfma32(ka, qa[1], negm[1]); sa1 = mfma8(kb, qb4[1], sa1);
  }
#define STEP(c0, c1, n0, n1, AK2, AKB2, AV1, LAST) do { \
    bf16x8 ka2, nv0, nv1; bf16x4 kb2; \
    if (!(LAST)) n0 = mfma32(kaN, qa[0], negm[0]); \
    EXP4(c0, 0); \
    if (!(LAST)) { ka2 = lds_b128(AK2); kb2 = lds_b64(AKB2); } \
    SB(); \
    if (!(LAST)) n0 = mfma8(kbN, qb4[0], n0); \
    EXP4(c0, 4); \
    SB(); \
    o0 = pv_lo(v0, c0, o0); \
    EXP4(c0, 8); \
    if (!(LAST)) { nv0 = lds_b128(AV1); } \
    SB(); \
    if (!(LAST)) n1 = mfma32(kaN, qa[1], negm[1]); \
    EXP4(c0, 12); \
    if (!(LAST)) { nv1 = lds_b128((AV1) + 672); } \
    SB(); \
    o0 = pv_hi(v1, c0, o0); \
    EXP4(c1, 0); \
    SB(); \
    if (!(LAST)) n1 = mfma8(kbN, qb4[1], n1); \
    EXP4(c1, 4); \
    SB(); \
    o1 = pv_lo(v0, c1, o1); \
    EXP4(c1, 8); \
    SB(); \
    EXP4(c1, 12); \
    SB(); \
    o1 = pv_hi(v1, c1, o1); \
    if (!(LAST)) { kaN = ka2; kbN = kb2; v0 = nv0; v1 = nv1; } \
  } while (0)

  for (int c = 0; c < 4; ++c) {
    const unsigned nxt = ldsh + ((c + 1) % 3) * K2_SLOT;
    STEP(sa0, sa1, sb0, sb1, cur + aK + 2 * 1024, cur + aKB + 2 * 256, cur + aV + 2 * 672, false);
    STEP(sb0, sb1, sa0, sa1, cur + aK + 3 * 1024, cur + aKB + 3 * 256, cur + aV + 4 * 672, false);
    STEP(sa0, sa1, sb0, sb1, cur + aK + 4 * 1024, cur + aKB + 4 * 256, cur + aV + 6 * 672, false);
    STEP(sb0, sb1, sa0, sa1, cur + aK + 5 * 1024, cur + aKB + 5 * 256, cur + aV + 8 * 672, false);
    STEP(sa0, sa1, sb0, sb1, cur + aK + 6 * 1024, cur + aKB + 6 * 256, cur + aV + 10 * 672, false);
    STEP(sb0, sb1, sa0, sa1, cur + aK + 7 * 1024, cur + aKB + 7 * 256, cur + aV + 12 * 672, false);
    if (c < 3) {
      WAITV(0); BARRIER();
      if (c == 1) stage(3);
      STEP(sa0, sa1, sb0, sb1, nxt + aK, nxt + aKB, cur + aV + 14 * 672, false);
      STEP(sb0, sb1, sa0, sa1, nxt + aK + 1024, nxt + aKB + 256, nxt + aV, false);
    } else {
      STEP(sa0, sa1, sb0, sb1, cur + aK, cur + aKB, cur + aV + 14 * 672, false);
      STEP(sb0, sb1, sa0, sa1, cur, cur, cur, true);
    }
    cur = nxt;
  }
  BARRIER();
  {
    float* ex = nullptr; (void)ex;
    const unsigned xa = (rg * 2 + kh) * (12 * 256) + lane * 4;
    f32x16 give = kh ? o0 : o1;
#pragma unroll
    for (int r = 0; r < 12; ++r) *(LDSP float*)(size_t)(xa + r * 256) = give[r];
    BARRIER();
    const unsigned xb = (rg * 2 + (kh ^ 1)) * (12 * 256) + lane * 4;
    f32x16 mine = kh ? o1 : o0;
#pragma unroll
    for (int r = 0; r < 12; ++r) mine[r] += *(LDSP const float*)(size_t)(xb + r * 256);
    const float l = __shfl(mine[8], l31 + 32);
    const float inv = 1.0f / l;
    bf16_t* o = (bf16_t*)(ws + WS_O) + (size_t)(q0 + kh * 32 + l31) * D + head * DH + 4 * hh;
    *(u32x2*)(o) = u32x2{pk2(mine[0] * inv, mine[1] * inv), pk2(mine[2] * inv, mine[3] * inv)};
    *(u32x2*)(o + 8) = u32x2{pk2(mine[4] * inv, mine[5] * inv), pk2(mine[6] * inv, mine[7] * inv)};
    if (hh == 0) *(u32x2*)(o + 16) = u32x2{pk2(mine[8] * inv, mine[9] * inv), pk2(mine[10] * inv, mine[11] * inv)};
  }
}

constexpr int K3_NST = 6, K3_W = 8192, K3_STAGE = 18432, K3_LDS = K3_NST * K3_STAGE, K3_PD = K3_NST - 1;
typedef float f32x4v __attribute__((ext_vector_type(4)));
__global__ __launch_bounds__(512, 2) void k3_out(const bf16_t* __restrict__ wo, const bf16_t* __restrict__ wd, const char* __restrict__ ws, bf16_t* __restrict__ out) {
  const unsigned tid = threadIdx.x, lane = tid & 63, wid = rfl(tid >> 6);
  const int b = blockIdx.x, xcd = b & 7, idx = b >> 3;
  const int rb = (xcd >> 1) * 8 + (idx & 7), cbk = (xcd & 1) * 4 + (idx >> 3);
  const int row0 = rb * 64, col0 = cbk * 80;
  const char* og = ws + WS_O + (size_t)row0 * D * 2;
  const char* hg = ws + WS_H + (size_t)row0 * F * 2;
  unsigned vxo, vxh;
  { unsigned row = wid * 8 + (lane >> 3), c = (lane & 7) ^ ((row >> 1) & 7); vxo = row * (D * 2) + c * 16; vxh = row * (F * 2) + c * 16; }
  unsigned vw0, vw1;
  { unsigned i = wid * 64 + lane; vw0 = (i / 10) * (D * 2) + (i % 10) * 16; i += 512; vw1 = (i / 10) * (D * 2) + (i % 10) * 16; }
  const char* wob = (const char*)(wo + col0); const char* wdb = (const char*)(wd + col0);
  auto stage = [&](int s) {
    const unsigned base = (s % K3_NST) * K3_STAGE;
    const char* wb = s < 10 ? wob + (size_t)s * 64 * D * 2 : wdb + (size_t)(s - 10) * 64 * D * 2;
    if (wid < 2) dma16(wb, vw1, base + K3_W + 8192 + wid * 1024);
    dma16(wb, vw0, base + K3_W + wid * 1024);
    if (s < 10) dma16(og + s * 128, vxo, base + wid * 1024); else dma16(hg + (s - 10) * 128, vxh, base + wid * 1024);
  };
#pragma unroll
  for (int s = 0; s < K3_PD; ++s) stage(s);
  const unsigned wm = wid & 3, kq = wid >> 2, l15 = lane & 15, g4 = lane >> 4;
  const unsigned axr = (wm * 16 + l15) * 128 + (((kq * 4 + g4) ^ (((wm * 16 + l15) >> 1) & 7)) << 4);
  const unsigned awr = K3_W + (kq * 32 + 8 * g4 + (l15 >> 2)) * 160 + (lane & 3) * 8;
  f32x4v acc[5] = {}, accA[5];
  for (int s = 0; s < 37; ++s) {
    if (s + K3_PD <= 37) { if (wid < 2) WAITV(12); else WAITV(8); }
    else WAITV(0);
    BARRIER();
    if (s + K3_PD < 37) stage(s + K3_PD);
    const unsigned sb = (s % K3_NST) * K3_STAGE;
    bf16x8 xf = lds_b128(sb + axr);
#pragma unroll
    for (int n = 0; n < 5; ++n) {
      bf16x8 wf = cat4(lds_tr(sb + awr + n * 32), lds_tr(sb + awr + n * 32 + 4 * 160));
      acc[n] = __builtin_amdgcn_mfma_f32_16x16x32_bf16(wf, xf, acc[n], 0, 0, 0);
    }
    if (s == 9) {
#pragma unroll
      for (int n = 0; n < 5; ++n) { accA[n] = acc[n]; acc[n] = f32x4v{}; }
    }
  }
  const float g = ((const float*)(ws + WS_G))[row0 + wm * 16 + l15];
  BARRIER();
  f32x4v res[5];
#pragma unroll
  for (int n = 0; n < 5; ++n) res[n] = accA[n] * g + acc[n] * (1.0f - g);
  const unsigned xa = (wm * 5) * 1024 + lane * 16;
  if (kq == 1) {
#pragma unroll
    for (int n = 0; n < 5; ++n) *(LDSP f32x4v*)(size_t)(xa + n * 1024) = res[n];
  }
  BARRIER();
  if (kq == 0) {
#pragma unroll
    for (int n = 0; n < 5; ++n) {
      f32x4v r = res[n] + *(LDSP const f32x4v*)(size_t)(xa + n * 1024);
      *(u32x2*)(out + (size_t)(row0 + wm * 16 + l15) * D + col0 + n * 16 + g4 * 4) = u32x2{pk2(r[0], r[1]), pk2(r[2], r[3])};
    }
  }
}

extern "C" void kernel_launch(void* const* d_in, const int* in_sizes, int n_in, void* d_out, int out_size, void* d_ws, size_t ws_size, hipStream_t stream) {
  const bf16_t* x = (const bf16_t*)d_in[0];
  const bf16_t* wq = (const bf16_t*)d_in[1];
  const bf16_t* wk = (const bf16_t*)d_in[2];
  const bf16_t* wv = (const bf16_t*)d_in[3];
  const bf16_t* wo = (const bf16_t*)d_in[4];
  const bf16_t* wg = (const bf16_t*)d_in[6];
  const bf16_t* wu = (const bf16_t*)d_in[7];
  const bf16_t* wd = (const bf16_t*)d_in[8];
  const bf16_t* gw = (const bf16_t*)d_in[9];
  char* ws = (char*)d_ws;
  (void)hipFuncSetAttribute((const void*)k1_proj, hipFuncAttributeMaxDynamicSharedMemorySize, K1_LDS);
  (void)hipFuncSetAttribute((const void*)k2_attn, hipFuncAttributeMaxDynamicSharedMemorySize, K2_LDS);
  (void)hipFuncSetAttribute((const void*)k3_out, hipFuncAttributeMaxDynamicSharedMemorySize, K3_LDS);
  k1_proj<<<256, 512, K1_LDS, stream>>>(x, wq, wk, wv, wg, wu, gw, ws);
  k2_attn<<<256, 512, K2_LDS, stream>>>(ws);
  k3_out<<<256, 512, K3_LDS, stream>>>(wo, wd, ws, (bf16_t*)d_out);
}
```

```cpp
#include <hip/hip_runtime.h>
#include <hip/hip_bf16.h>
#include <stdint.h>

typedef __bf16 bf16_t;
typedef __bf16 bf16x2 __attribute__((ext_vector_type(2)));
typedef __bf16 bf16x4 __attribute__((ext_vector_type(4)));
typedef __bf16 bf16x8 __attribute__((ext_vector_type(8)));
typedef __bf16 bf16x16 __attribute__((ext_vector_type(16)));
typedef short s16x4 __attribute__((ext_vector_type(4)));
typedef float f32x2 __attribute__((ext_vector_type(2)));
typedef float f32x4 __attribute__((ext_vector_type(4)));
typedef float f32x8 __attribute__((ext_vector_type(8)));
typedef float f32x16 __attribute__((ext_vector_type(16)));
typedef unsigned u32x2 __attribute__((ext_vector_type(2)));
typedef unsigned u32x4 __attribute__((ext_vector_type(4)));
#define LDSP __attribute__((address_space(3)))
#define GASP __attribute__((address_space(1)))
#define DI __device__ __forceinline__
typedef __amdgpu_buffer_rsrc_t rsrc_t;

constexpr int S = 2048, D = 640, H = 32, DH = 20, F = 1728;
constexpr float QSCALE = 0.22360679774997896f * 1.4426950408889634f;

constexpr unsigned WS_HF = 0;
constexpr unsigned WS_WP = WS_HF + 64 * 108 * 1024;
constexpr unsigned WS_WQ = WS_WP + 8 * 148 * 3 * 1024;
constexpr unsigned WS_OF = WS_WQ + 8 * 80 * 2560;
constexpr int KV_CHUNK = 24576;
constexpr int KV_KB = 8192, KV_V = 12288;
constexpr unsigned WS_KV = WS_OF + 64 * 40 * 1024;
constexpr unsigned WS_G = WS_KV + 8 * KV_CHUNK;
constexpr unsigned WS_KM = WS_G + S * 4;
constexpr unsigned WS_EPOCH = WS_KM + 256;
constexpr unsigned WS_FARR = WS_EPOCH + 128;
constexpr unsigned WS_FKV = WS_FARR + 256 * 128;
constexpr unsigned WS_FH = WS_FKV + 128 * 128;
constexpr unsigned WS_FW = WS_FH + 216 * 128;
constexpr unsigned WS_FWQ = WS_FW + 148 * 128;
constexpr unsigned WS_END = WS_FWQ + 40 * 128;

DI unsigned pk2(float lo, float hi) { f32x2 v = {lo, hi}; bf16x2 b = __builtin_convertvector(v, bf16x2); return __builtin_bit_cast(unsigned, b); }
DI unsigned rfl(unsigned x) { return __builtin_amdgcn_readfirstlane(x); }
DI float wave_max_pos(float v) {
  unsigned x = __float_as_uint(v), y;
  y = __builtin_amdgcn_update_dpp(0u, x, 0x111, 0xf, 0xf, true); x = x > y ? x : y;
  y = __builtin_amdgcn_update_dpp(0u, x, 0x112, 0xf, 0xf, true); x = x > y ? x : y;
  y = __builtin_amdgcn_update_dpp(0u, x, 0x114, 0xf, 0xf, true); x = x > y ? x : y;
  y = __builtin_amdgcn_update_dpp(0u, x, 0x118, 0xf, 0xf, true); x = x > y ? x : y;
  y = __builtin_amdgcn_update_dpp(0u, x, 0x142, 0xa, 0xf, true); x = x > y ? x : y;
  y = __builtin_amdgcn_update_dpp(0u, x, 0x143, 0xc, 0xf, true); x = x > y ? x : y;
  return __uint_as_float(__builtin_amdgcn_readlane(x, 63));
}
DI float half_sum(float v) { auto r = __builtin_amdgcn_permlane32_swap(__float_as_uint(v), __float_as_uint(v), false, false); return __uint_as_float(r[0]) + __uint_as_float(r[1]); }
DI unsigned fresh_lane() { unsigned l; asm volatile("v_mbcnt_lo_u32_b32 %0, -1, 0\n\tv_mbcnt_hi_u32_b32 %0, -1, %0" : "=v"(l)); return l; }
DI void dma16(const void* sbase, unsigned voff, unsigned ldsaddr) {
  asm volatile("s_mov_b32 m0, %2\n\ts_nop 0\n\tglobal_load_lds_dwordx4 %0, %1" :: "v"(voff), "s"(sbase), "s"(ldsaddr) : "memory");
}
#define WAITV(n) asm volatile("s_waitcnt vmcnt(" #n ")" ::: "memory")
#define BARRIER() do { asm volatile("s_waitcnt lgkmcnt(0)" ::: "memory"); __builtin_amdgcn_s_barrier(); asm volatile("" ::: "memory"); } while (0)
#define SB() __builtin_amdgcn_sched_barrier(0)
DI bf16x8 lds_b128(unsigned a) { return *(LDSP const bf16x8*)(size_t)a; }
DI bf16x4 lds_tr(unsigned a) { return __builtin_amdgcn_ds_read_tr16_b64_v4bf16((LDSP bf16x4*)(size_t)a); }
DI bf16x8 cat4(bf16x4 a, bf16x4 b) { return __builtin_shufflevector(a, b, 0, 1, 2, 3, 4, 5, 6, 7); }
DI f32x16 mfma32(bf16x8 a, bf16x8 b, f32x16 c) { return __builtin_amdgcn_mfma_f32_32x32x16_bf16(a, b, c, 0, 0, 0); }
DI f32x16 mfma8(bf16x4 a, bf16x4 b, f32x16 c) { return __builtin_amdgcn_mfma_f32_32x32x8bf16_1k(__builtin_bit_cast(s16x4, a), __builtin_bit_cast(s16x4, b), c, 0, 0, 0); }
DI rsrc_t make_rsrc(const void* p, unsigned bytes) { return __builtin_amdgcn_make_buffer_rsrc((void*)p, 0, bytes, 0x00020000); }
constexpr int SC1 = 16;
DI bf16x8 bld128(rsrc_t r, unsigned voff, unsigned soff) { return __builtin_bit_cast(bf16x8, __builtin_amdgcn_raw_buffer_load_b128(r, voff, soff, SC1)); }
DI bf16x4 bld64(rsrc_t r, unsigned voff, unsigned soff) { return __builtin_bit_cast(bf16x4, __builtin_amdgcn_raw_buffer_load_b64(r, voff, soff, SC1)); }
DI float bld32f(rsrc_t r, unsigned voff, unsigned soff) { return __uint_as_float(__builtin_amdgcn_raw_buffer_load_b32(r, voff, soff, SC1)); }
DI void bst128(u32x4 v, rsrc_t r, unsigned voff, unsigned soff) { __builtin_amdgcn_raw_buffer_store_b128(v, r, voff + soff, 0, SC1); }
DI void bst64(u32x2 v, rsrc_t r, unsigned voff, unsigned soff) { __builtin_amdgcn_raw_buffer_store_b64(v, r, voff, soff, SC1); }
DI void bst32(unsigned v, rsrc_t r, unsigned voff, unsigned soff) { __builtin_amdgcn_raw_buffer_store_b32(v, r, voff, soff, SC1); }
DI void dma16b(rsrc_t r, unsigned voff, unsigned soff, unsigned ldsaddr) {
  asm volatile("s_mov_b32 m0, %3\n\ts_nop 0\n\tbuffer_load_dwordx4 %0, %1, %2 offen sc1 lds" :: "v"(voff), "s"(r), "s"(soff), "s"(ldsaddr) : "memory");
}
typedef GASP unsigned gu32;
DI void flag_set(char* ws, unsigned off, unsigned tag) { __hip_atomic_store((gu32*)(ws + off), tag, __ATOMIC_RELAXED, __HIP_MEMORY_SCOPE_AGENT); }
DI unsigned flag_get(const char* ws, unsigned off) { return __hip_atomic_load((gu32*)(ws + off), __ATOMIC_RELAXED, __HIP_MEMORY_SCOPE_AGENT); }
DI void poll_flags(const char* ws, unsigned off, bool act, unsigned tag) {
  for (unsigned spins = 0; spins < (1u << 20); ++spins) {
    const unsigned v = act ? flag_get(ws, off) : tag;
    if (__all(v == tag)) break;
    __builtin_amdgcn_s_sleep(2);
  }
  __builtin_amdgcn_fence(__ATOMIC_ACQUIRE, "wavefront");
}

constexpr int K1_X = 0, K1_WL = 32768, K1_WH = 40960, K1_STAGE = 52224;
struct AOut { f32x16 acc[2][2]; };

#define GEMM_PLAIN \
  "s_mov_b64 s[74:75], %[xin_]\n\t" \
  "s_mov_b64 s[76:77], %[wlin_]\n\t" \
  "s_mov_b64 s[78:79], %[whin_]\n\t" \
  "s_lshl_b32 %[st], %[sk], 7\n\t" \
  "s_add_u32 s60, s74, %[st]\n\t" \
  "s_addc_u32 s61, s75, 0\n\t" \
  "s_mul_i32 %[st], %[sk], %[wst]\n\t" \
  "s_add_u32 s62, s76, %[st]\n\t" \
  "s_addc_u32 s63, s77, 0\n\t" \
  "s_add_u32 s64, s78, %[st]\n\t" \
  "s_addc_u32 s65, s79, 0\n\t" \
  "s_mov_b64 s[68:69], %[qxin]\n\t" \
  "buffer_load_dword v155, off, %[srd], %[epo] sc1\n\t" \
  "v_mov_b32_e32 v150, -1\n\t" \
  "v_mov_b32_e32 v151, -1\n\t" \
  "v_mov_b32_e32 v152, -1\n\t" \
  "v_mov_b32_e32 v153, -1\n\t" \
  "ds_read_b128 v[112:115], v160\n\t" \
  "ds_read_b128 v[116:119], v160 offset:4096\n\t" \
  "ds_read_b64_tr_b16 v[120:121], v172 offset:0\n\t" \
  "ds_read_b64_tr_b16 v[122:123], v172 offset:512\n\t" \
  "ds_read_b64_tr_b16 v[124:125], v172 offset:8192\n\t" \
  "ds_read_b64_tr_b16 v[126:127], v172 offset:8704\n\t" \
  "s_waitcnt lgkmcnt(2)\n\t" \
  "v_mfma_f32_32x32x16_bf16 v[32:47], v[120:123], v[112:115], 0\n\t" \
  "ds_read_b128 v[128:131], v161\n\t" \
  "ds_read_b128 v[132:135], v161 offset:4096\n\t" \
  "v_mfma_f32_32x32x16_bf16 v[48:63], v[120:123], v[116:119], 0\n\t" \
  "ds_read_b64_tr_b16 v[136:137], v172 offset:2048\n\t" \
  "ds_read_b64_tr_b16 v[138:139], v172 offset:2560\n\t" \
  "s_waitcnt lgkmcnt(4)\n\t" \
  "v_mfma_f32_32x32x16_bf16 v[64:79], v[124:127], v[112:115], 0\n\t" \
  "ds_read_b64_tr_b16 v[140:141], v172 offset:10240\n\t" \
  "ds_read_b64_tr_b16 v[142:143], v172 offset:10752\n\t" \
  "s_add_u32 m0, %[mw], 104448\n\t" \
  "s_nop 0\n\t" \
  "global_load_lds_dwordx4 v108, s[62:63]\n\t" \
  "v_mfma_f32_32x32x16_bf16 v[80:95], v[124:127], v[116:119], 0\n\t" \
  "s_add_u32 m0, %[mw], 112640\n\t" \
  "s_nop 0\n\t" \
  "global_load_lds_dwordx4 v108, s[64:65]\n\t" \
  "s_waitcnt lgkmcnt(2)\n\t" \
  "v_mfma_f32_32x32x16_bf16 v[32:47], v[136:139], v[128:131], v[32:47]\n\t" \
  "ds_read_b128 v[112:115], v162\n\t" \
  "ds_read_b128 v[116:119], v162 offset:4096\n\t" \
  "v_mfma_f32_32x32x16_bf16 v[48:63], v[136:139], v[132:135], v[48:63]\n\t" \
  "ds_read_b64_tr_b16 v[120:121], v172 offset:4096\n\t" \
  "ds_read_b64_tr_b16 v[122:123], v172 offset:4608\n\t" \
  "s_waitcnt lgkmcnt(4)\n\t" \
  "v_mfma_f32_32x32x16_bf16 v[64:79], v[140:143], v[128:131], v[64:79]\n\t" \
  "ds_read_b64_tr_b16 v[124:125], v172 offset:12288\n\t" \
  "ds_read_b64_tr_b16 v[126:127], v172 offset:12800\n\t" \
  "s_add_u32 m0, %[mx], 104448\n\t" \
  "s_nop 0\n\t" \
  "global_load_lds_dwordx4 v104, s[60:61]\n\t" \
  "v_mfma_f32_32x32x16_bf16 v[80:95], v[140:143], v[132:135], v[80:95]\n\t" \
  "s_add_u32 m0, %[mx], 105472\n\t" \
  "s_nop 0\n\t" \
  "global_load_lds_dwordx4 v105, s[60:61]\n\t" \
  "s_waitcnt lgkmcnt(2)\n\t" \
  "v_mfma_f32_32x32x16_bf16 v[32:47], v[120:123], v[112:115], v[32:47]\n\t" \
  "ds_read_b128 v[128:131], v163\n\t" \
  "ds_read_b128 v[132:135], v163 offset:4096\n\t" \
  "v_mfma_f32_32x32x16_bf16 v[48:63], v[120:123], v[116:119], v[48:63]\n\t" \
  "ds_read_b64_tr_b16 v[136:137], v172 offset:6144\n\t" \
  "ds_read_b64_tr_b16 v[138:139], v172 offset:6656\n\t" \
  "s_waitcnt lgkmcnt(4)\n\t" \
  "v_mfma_f32_32x32x16_bf16 v[64:79], v[124:127], v[112:115], v[64:79]\n\t" \
  "ds_read_b64_tr_b16 v[140:141], v172 offset:14336\n\t" \
  "ds_read_b64_tr_b16 v[142:143], v172 offset:14848\n\t" \
  "s_add_u32 m0, %[mx], 106496\n\t" \
  "s_nop 0\n\t" \
  "global_load_lds_dwordx4 v106, s[60:61]\n\t" \
  "v_mfma_f32_32x32x16_bf16 v[80:95], v[124:127], v[116:119], v[80:95]\n\t" \
  "s_add_u32 m0, %[mx], 107520\n\t" \
  "s_nop 0\n\t" \
  "global_load_lds_dwordx4 v107, s[60:61]\n\t" \
  "s_waitcnt lgkmcnt(2)\n\t" \
  "v_mfma_f32_32x32x16_bf16 v[32:47], v[136:139], v[128:131], v[32:47]\n\t" \
  "v_mfma_f32_32x32x16_bf16 v[48:63], v[136:139], v[132:135], v[48:63]\n\t" \
  "s_waitcnt vmcnt(7) lgkmcnt(0)\n\t" \
  "s_barrier\n\t" \
  "ds_read_b128 v[112:115], v164\n\t" \
  "ds_read_b128 v[116:119], v164 offset:4096\n\t" \
  "ds_read_b64_tr_b16 v[120:121], v173 offset:0\n\t" \
  "ds_read_b64_tr_b16 v[122:123], v173 offset:512\n\t" \
  "v_mfma_f32_32x32x16_bf16 v[64:79], v[140:143], v[128:131], v[64:79]\n\t" \
  "ds_read_b64_tr_b16 v[124:125], v173 offset:8192\n\t" \
  "ds_read_b64_tr_b16 v[126:127], v173 offset:8704\n\t" \
  "v_mfma_f32_32x32x16_bf16 v[80:95], v[140:143], v[132:135], v[80:95]\n\t" \
  "s_add_u32 %[sk], %[sk], 1\n\t" \
  "s_cmp_eq_u32 %[sk], 10\n\t" \
  "s_cselect_b32 %[sk], 0, %[sk]\n\t" \
  "s_lshl_b32 %[st], %[sk], 7\n\t" \
  "s_add_u32 s60, s74, %[st]\n\t" \
  "s_addc_u32 s61, s75, 0\n\t" \
  "s_mul_i32 %[st], %[sk], %[wst]\n\t" \
  "s_add_u32 s62, s76, %[st]\n\t" \
  "s_addc_u32 s63, s77, 0\n\t" \
  "s_add_u32 s64, s78, %[st]\n\t" \
  "s_addc_u32 s65, s79, 0\n\t" \
  "s_waitcnt lgkmcnt(2)\n\t" \
  "v_mfma_f32_32x32x16_bf16 v[32:47], v[120:123], v[112:115], v[32:47]\n\t" \
  "ds_read_b128 v[128:131], v165\n\t" \
  "ds_read_b128 v[132:135], v165 offset:4096\n\t" \
  "v_mfma_f32_32x32x16_bf16 v[48:63], v[120:123], v[116:119], v[48:63]\n\t" \
  "ds_read_b64_tr_b16 v[136:137], v173 offset:2048\n\t" \
  "ds_read_b64_tr_b16 v[138:139], v173 offset:2560\n\t" \
  "s_waitcnt lgkmcnt(4)\n\t" \
  "v_mfma_f32_32x32x16_bf16 v[64:79], v[124:127], v[112:115], v[64:79]\n\t" \
  "ds_read_b64_tr_b16 v[140:141], v173 offset:10240\n\t" \
  "ds_read_b64_tr_b16 v[142:143], v173 offset:10752\n\t" \
  "s_add_u32 m0, %[mw], 0\n\t" \
  "s_nop 0\n\t" \
  "global_load_lds_dwordx4 v108, s[62:63]\n\t" \
  "v_mfma_f32_32x32x16_bf16 v[80:95], v[124:127], v[116:119], v[80:95]\n\t" \
  "s_add_u32 m0, %[mw], 8192\n\t" \
  "s_nop 0\n\t" \
  "global_load_lds_dwordx4 v108, s[64:65]\n\t" \
  "s_waitcnt lgkmcnt(2)\n\t" \
  "v_mfma_f32_32x32x16_bf16 v[32:47], v[136:139], v[128:131], v[32:47]\n\t" \
  "ds_read_b128 v[112:115], v166\n\t" \
  "ds_read_b128 v[116:119], v166 offset:4096\n\t" \
  "v_mfma_f32_32x32x16_bf16 v[48:63], v[136:139], v[132:135], v[48:63]\n\t" \
  "ds_read_b64_tr_b16 v[120:121], v173 offset:4096\n\t" \
  "ds_read_b64_tr_b16 v[122:123], v173 offset:4608\n\t" \
  "s_waitcnt lgkmcnt(4)\n\t" \
  "v_mfma_f32_32x32x16_bf16 v[64:79], v[140:143], v[128:131], v[64:79]\n\t" \
  "ds_read_b64_tr_b16 v[124:125], v173 offset:12288\n\t" \
  "ds_read_b64_tr_b16 v[126:127], v173 offset:12800\n\t" \
  "s_add_u32 m0, %[mx], 0\n\t" \
  "s_nop 0\n\t" \
  "global_load_lds_dwordx4 v104, s[60:61]\n\t" \
  "v_mfma_f32_32x32x16_bf16 v[80:95], v[140:143], v[132:135], v[80:95]\n\t" \
  "s_add_u32 m0, %[mx], 1024\n\t" \
  "s_nop 0\n\t" \
  "global_load_lds_dwordx4 v105, s[60:61]\n\t" \
  "s_waitcnt lgkmcnt(2)\n\t" \
  "v_mfma_f32_32x32x16_bf16 v[32:47], v[120:123], v[112:115], v[32:47]\n\t" \
  "ds_read_b128 v[128:131], v167\n\t" \
  "ds_read_b128 v[132:135], v167 offset:4096\n\t" \
  "v_mfma_f32_32x32x16_bf16 v[48:63], v[120:123], v[116:119], v[48:63]\n\t" \
  "ds_read_b64_tr_b16 v[136:137], v173 offset:6144\n\t" \
  "ds_read_b64_tr_b16 v[138:139], v173 offset:6656\n\t" \
  "s_waitcnt lgkmcnt(4)\n\t" \
  "v_mfma_f32_32x32x16_bf16 v[64:79], v[124:127], v[112:115], v[64:79]\n\t" \
  "ds_read_b64_tr_b16 v[140:141], v173 offset:14336\n\t" \
  "ds_read_b64_tr_b16 v[142:143], v173 offset:14848\n\t" \
  "s_add_u32 m0, %[mx], 2048\n\t" \
  "s_nop 0\n\t" \
  "global_load_lds_dwordx4 v106, s[60:61]\n\t" \
  "v_mfma_f32_32x32x16_bf16 v[80:95], v[124:127], v[116:119], v[80:95]\n\t" \
  "s_add_u32 m0, %[mx], 3072\n\t" \
  "s_nop 0\n\t" \
  "global_load_lds_dwordx4 v107, s[60:61]\n\t" \
  "s_waitcnt lgkmcnt(2)\n\t" \
  "v_mfma_f32_32x32x16_bf16 v[32:47], v[136:139], v[128:131], v[32:47]\n\t" \
  "v_mfma_f32_32x32x16_bf16 v[48:63], v[136:139], v[132:135], v[48:63]\n\t" \
  "s_waitcnt vmcnt(6) lgkmcnt(0)\n\t" \
  "s_barrier\n\t" \
  "ds_read_b128 v[112:115], v168\n\t" \
  "ds_read_b128 v[116:119], v168 offset:4096\n\t" \
  "ds_read_b64_tr_b16 v[120:121], v174 offset:0\n\t" \
  "ds_read_b64_tr_b16 v[122:123], v174 offset:512\n\t" \
  "v_mfma_f32_32x32x16_bf16 v[64:79], v[140:143], v[128:131], v[64:79]\n\t" \
  "ds_read_b64_tr_b16 v[124:125], v174 offset:8192\n\t" \
  "ds_read_b64_tr_b16 v[126:127], v174 offset:8704\n\t" \
  "v_mfma_f32_32x32x16_bf16 v[80:95], v[140:143], v[132:135], v[80:95]\n\t" \
  "s_add_u32 %[sk], %[sk], 1\n\t" \
  "s_cmp_eq_u32 %[sk], 10\n\t" \
  "s_cselect_b32 %[sk], 0, %[sk]\n\t" \
  "s_lshl_b32 %[st], %[sk], 7\n\t" \
  "s_add_u32 s60, s74, %[st]\n\t" \
  "s_addc_u32 s61, s75, 0\n\t" \
  "s_mul_i32 %[st], %[sk], %[wst]\n\t" \
  "s_add_u32 s62, s76, %[st]\n\t" \
  "s_addc_u32 s63, s77, 0\n\t" \
  "s_add_u32 s64, s78, %[st]\n\t" \
  "s_addc_u32 s65, s79, 0\n\t" \
  "s_waitcnt lgkmcnt(2)\n\t" \
  "v_mfma_f32_32x32x16_bf16 v[32:47], v[120:123], v[112:115], v[32:47]\n\t" \
  "ds_read_b128 v[128:131], v169\n\t" \
  "ds_read_b128 v[132:135], v169 offset:4096\n\t" \
  "v_mfma_f32_32x32x16_bf16 v[48:63], v[120:123], v[116:119], v[48:63]\n\t" \
  "ds_read_b64_tr_b16 v[136:137], v174 offset:2048\n\t" \
  "ds_read_b64_tr_b16 v[138:139], v174 offset:2560\n\t" \
  "s_waitcnt lgkmcnt(4)\n\t" \
  "v_mfma_f32_32x32x16_bf16 v[64:79], v[124:127], v[112:115], v[64:79]\n\t" \
  "ds_read_b64_tr_b16 v[140:141], v174 offset:10240\n\t" \
  "ds_read_b64_tr_b16 v[142:143], v174 offset:10752\n\t" \
  "s_add_u32 m0, %[mw], 52224\n\t" \
  "s_nop 0\n\t" \
  "global_load_lds_dwordx4 v108, s[62:63]\n\t" \
  "v_mfma_f32_32x32x16_bf16 v[80:95], v[124:127], v[116:119], v[80:95]\n\t" \
  "s_add_u32 m0, %[mw], 60416\n\t" \
  "s_nop 0\n\t" \
  "global_load_lds_dwordx4 v108, s[64:65]\n\t" \
  "s_waitcnt lgkmcnt(2)\n\t" \
  "v_mfma_f32_32x32x16_bf16 v[32:47], v[136:139], v[128:131], v[32:47]\n\t" \
  "ds_read_b128 v[112:115], v170\n\t" \
  "ds_read_b128 v[116:119], v170 offset:4096\n\t" \
  "v_mfma_f32_32x32x16_bf16 v[48:63], v[136:139], v[132:135], v[48:63]\n\t" \
  "ds_read_b64_tr_b16 v[120:121], v174 offset:4096\n\t" \
  "ds_read_b64_tr_b16 v[122:123], v174 offset:4608\n\t" \
  "s_waitcnt lgkmcnt(4)\n\t" \
  "v_mfma_f32_32x32x16_bf16 v[64:79], v[140:143], v[128:131], v[64:79]\n\t" \
  "ds_read_b64_tr_b16 v[124:125], v174 offset:12288\n\t" \
  "ds_read_b64_tr_b16 v[126:127], v174 offset:12800\n\t" \
  "s_add_u32 m0, %[mx], 52224\n\t" \
  "s_nop 0\n\t" \
  "global_load_lds_dwordx4 v104, s[60:61]\n\t" \
  "v_mfma_f32_32x32x16_bf16 v[80:95], v[140:143], v[132:135], v[80:95]\n\t" \
  "s_add_u32 m0, %[mx], 53248\n\t" \
  "s_nop 0\n\t" \
  "global_load_lds_dwordx4 v105, s[60:61]\n\t" \
  "s_waitcnt lgkmcnt(2)\n\t" \
  "v_mfma_f32_32x32x16_bf16 v[32:47], v[120:123], v[112:115], v[32:47]\n\t" \
  "ds_read_b128 v[128:131], v171\n\t" \
  "ds_read_b128 v[132:135], v171 offset:4096\n\t" \
  "v_mfma_f32_32x32x16_bf16 v[48:63], v[120:123], v[116:119], v[48:63]\n\t" \
  "ds_read_b64_tr_b16 v[136:137], v174 offset:6144\n\t" \
  "ds_read_b64_tr_b16 v[138:139], v174 offset:6656\n\t" \
  "s_waitcnt lgkmcnt(4)\n\t" \
  "v_mfma_f32_32x32x16_bf16 v[64:79], v[124:127], v[112:115], v[64:79]\n\t" \
  "ds_read_b64_tr_b16 v[140:141], v174 offset:14336\n\t" \
  "ds_read_b64_tr_b16 v[142:143], v174 offset:14848\n\t" \
  "s_add_u32 m0, %[mx], 54272\n\t" \
  "s_nop 0\n\t" \
  "global_load_lds_dwordx4 v106, s[60:61]\n\t" \
  "v_mfma_f32_32x32x16_bf16 v[80:95], v[124:127], v[116:119], v[80:95]\n\t" \
  "s_add_u32 m0, %[mx], 55296\n\t" \
  "s_nop 0\n\t" \
  "global_load_lds_dwordx4 v107, s[60:61]\n\t" \
  "buffer_store_dwordx4 v[150:153], v103, %[srd], %[mk0] offen sc1\n\t" \
  "buffer_store_dwordx4 v[150:153], v103, %[srd], %[mk1] offen sc1\n\t" \
  "s_waitcnt lgkmcnt(2)\n\t" \
  "v_mfma_f32_32x32x16_bf16 v[32:47], v[136:139], v[128:131], v[32:47]\n\t" \
  "v_mfma_f32_32x32x16_bf16 v[48:63], v[136:139], v[132:135], v[48:63]\n\t" \
  "s_waitcnt vmcnt(8) lgkmcnt(0)\n\t" \
  "s_barrier\n\t" \
  "ds_read_b128 v[112:115], v160\n\t" \
  "ds_read_b128 v[116:119], v160 offset:4096\n\t" \
  "ds_read_b64_tr_b16 v[120:121], v172 offset:0\n\t" \
  "ds_read_b64_tr_b16 v[122:123], v172 offset:512\n\t" \
  "v_mfma_f32_32x32x16_bf16 v[64:79], v[140:143], v[128:131], v[64:79]\n\t" \
  "ds_read_b64_tr_b16 v[124:125], v172 offset:8192\n\t" \
  "ds_read_b64_tr_b16 v[126:127], v172 offset:8704\n\t" \
  "v_mfma_f32_32x32x16_bf16 v[80:95], v[140:143], v[132:135], v[80:95]\n\t" \
  "s_add_u32 %[sk], %[sk], 1\n\t" \
  "s_cmp_eq_u32 %[sk], 10\n\t" \
  "s_cselect_b32 %[sk], 0, %[sk]\n\t" \
  "s_lshl_b32 %[st], %[sk], 7\n\t" \
  "s_add_u32 s60, s74, %[st]\n\t" \
  "s_addc_u32 s61, s75, 0\n\t" \
  "s_mul_i32 %[st], %[sk], %[wst]\n\t" \
  "s_add_u32 s62, s76, %[st]\n\t" \
  "s_addc_u32 s63, s77, 0\n\t" \
  "s_add_u32 s64, s78, %[st]\n\t" \
  "s_addc_u32 s65, s79, 0\n\t" \
  "s_waitcnt lgkmcnt(2)\n\t" \
  "v_mfma_f32_32x32x16_bf16 v[32:47], v[120:123], v[112:115], v[32:47]\n\t" \
  "ds_read_b128 v[128:131], v161\n\t" \
  "ds_read_b128 v[132:135], v161 offset:4096\n\t" \
  "v_mfma_f32_32x32x16_bf16 v[48:63], v[120:123], v[116:119], v[48:63]\n\t" \
  "ds_read_b64_tr_b16 v[136:137], v172 offset:2048\n\t" \
  "ds_read_b64_tr_b16 v[138:139], v172 offset:2560\n\t" \
  "s_waitcnt lgkmcnt(4)\n\t" \
  "v_mfma_f32_32x32x16_bf16 v[64:79], v[124:127], v[112:115], v[64:79]\n\t" \
  "ds_read_b64_tr_b16 v[140:141], v172 offset:10240\n\t" \
  "ds_read_b64_tr_b16 v[142:143], v172 offset:10752\n\t" \
  "s_add_u32 m0, %[mw], 104448\n\t" \
  "s_nop 0\n\t" \
  "global_load_lds_dwordx4 v108, s[62:63]\n\t" \
  "v_mfma_f32_32x32x16_bf16 v[80:95], v[124:127], v[116:119], v[80:95]\n\t" \
  "s_add_u32 m0, %[mw], 112640\n\t" \
  "s_nop 0\n\t" \
  "global_load_lds_dwordx4 v108, s[64:65]\n\t" \
  "s_waitcnt lgkmcnt(2)\n\t" \
  "v_mfma_f32_32x32x16_bf16 v[32:47], v[136:139], v[128:131], v[32:47]\n\t" \
  "ds_read_b128 v[112:115], v162\n\t" \
  "ds_read_b128 v[116:119], v162 offset:4096\n\t" \
  "v_mfma_f32_32x32x16_bf16 v[48:63], v[136:139], v[132:135], v[48:63]\n\t" \
  "ds_read_b64_tr_b16 v[120:121], v172 offset:4096\n\t" \
  "ds_read_b64_tr_b16 v[122:123], v172 offset:4608\n\t" \
  "s_waitcnt lgkmcnt(4)\n\t" \
  "v_mfma_f32_32x32x16_bf16 v[64:79], v[140:143], v[128:131], v[64:79]\n\t" \
  "ds_read_b64_tr_b16 v[124:125], v172 offset:12288\n\t" \
  "ds_read_b64_tr_b16 v[126:127], v172 offset:12800\n\t" \
  "s_add_u32 m0, %[mx], 104448\n\t" \
  "s_nop 0\n\t" \
  "global_load_lds_dwordx4 v104, s[60:61]\n\t" \
  "v_mfma_f32_32x32x16_bf16 v[80:95], v[140:143], v[132:135], v[80:95]\n\t" \
  "s_add_u32 m0, %[mx], 105472\n\t" \
  "s_nop 0\n\t" \
  "global_load_lds_dwordx4 v105, s[60:61]\n\t" \
  "s_waitcnt lgkmcnt(2)\n\t" \
  "v_mfma_f32_32x32x16_bf16 v[32:47], v[120:123], v[112:115], v[32:47]\n\t" \
  "ds_read_b128 v[128:131], v163\n\t" \
  "ds_read_b128 v[132:135], v163 offset:4096\n\t" \
  "v_mfma_f32_32x32x16_bf16 v[48:63], v[120:123], v[116:119], v[48:63]\n\t" \
  "ds_read_b64_tr_b16 v[136:137], v172 offset:6144\n\t" \
  "ds_read_b64_tr_b16 v[138:139], v172 offset:6656\n\t" \
  "s_waitcnt lgkmcnt(4)\n\t" \
  "v_mfma_f32_32x32x16_bf16 v[64:79], v[124:127], v[112:115], v[64:79]\n\t" \
  "ds_read_b64_tr_b16 v[140:141], v172 offset:14336\n\t" \
  "ds_read_b64_tr_b16 v[142:143], v172 offset:14848\n\t" \
  "s_add_u32 m0, %[mx], 106496\n\t" \
  "s_nop 0\n\t" \
  "global_load_lds_dwordx4 v106, s[60:61]\n\t" \
  "v_mfma_f32_32x32x16_bf16 v[80:95], v[124:127], v[116:119], v[80:95]\n\t" \
  "s_add_u32 m0, %[mx], 107520\n\t" \
  "s_nop 0\n\t" \
  "global_load_lds_dwordx4 v107, s[60:61]\n\t" \
  "s_waitcnt lgkmcnt(2)\n\t" \
  "v_mfma_f32_32x32x16_bf16 v[32:47], v[136:139], v[128:131], v[32:47]\n\t" \
  "v_mfma_f32_32x32x16_bf16 v[48:63], v[136:139], v[132:135], v[48:63]\n\t" \
  "s_waitcnt vmcnt(8) lgkmcnt(0)\n\t" \
  "s_barrier\n\t" \
  "ds_read_b128 v[112:115], v164\n\t" \
  "ds_read_b128 v[116:119], v164 offset:4096\n\t" \
  "ds_read_b64_tr_b16 v[120:121], v173 offset:0\n\t" \
  "ds_read_b64_tr_b16 v[122:123], v173 offset:512\n\t" \
  "v_mfma_f32_32x32x16_bf16 v[64:79], v[140:143], v[128:131], v[64:79]\n\t" \
  "ds_read_b64_tr_b16 v[124:125], v173 offset:8192\n\t" \
  "ds_read_b64_tr_b16 v[126:127], v173 offset:8704\n\t" \
  "v_mfma_f32_32x32x16_bf16 v[80:95], v[140:143], v[132:135], v[80:95]\n\t" \
  "s_add_u32 %[sk], %[sk], 1\n\t" \
  "s_cmp_eq_u32 %[sk], 10\n\t" \
  "s_cselect_b32 %[sk], 0, %[sk]\n\t" \
  "s_lshl_b32 %[st], %[sk], 7\n\t" \
  "s_add_u32 s60, s74, %[st]\n\t" \
  "s_addc_u32 s61, s75, 0\n\t" \
  "s_mul_i32 %[st], %[sk], %[wst]\n\t" \
  "s_add_u32 s62, s76, %[st]\n\t" \
  "s_addc_u32 s63, s77, 0\n\t" \
  "s_add_u32 s64, s78, %[st]\n\t" \
  "s_addc_u32 s65, s79, 0\n\t" \
  "s_waitcnt lgkmcnt(2)\n\t" \
  "v_mfma_f32_32x32x16_bf16 v[32:47], v[120:123], v[112:115], v[32:47]\n\t" \
  "ds_read_b128 v[128:131], v165\n\t" \
  "ds_read_b128 v[132:135], v165 offset:4096\n\t" \
  "v_mfma_f32_32x32x16_bf16 v[48:63], v[120:123], v[116:119], v[48:63]\n\t" \
  "ds_read_b64_tr_b16 v[136:137], v173 offset:2048\n\t" \
  "ds_read_b64_tr_b16 v[138:139], v173 offset:2560\n\t" \
  "s_waitcnt lgkmcnt(4)\n\t" \
  "v_mfma_f32_32x32x16_bf16 v[64:79], v[124:127], v[112:115], v[64:79]\n\t" \
  "ds_read_b64_tr_b16 v[140:141], v173 offset:10240\n\t" \
  "ds_read_b64_tr_b16 v[142:143], v173 offset:10752\n\t" \
  "s_add_u32 m0, %[mw], 0\n\t" \
  "s_nop 0\n\t" \
  "global_load_lds_dwordx4 v108, s[62:63]\n\t" \
  "v_mfma_f32_32x32x16_bf16 v[80:95], v[124:127], v[116:119], v[80:95]\n\t" \
  "s_add_u32 m0, %[mw], 8192\n\t" \
  "s_nop 0\n\t" \
  "global_load_lds_dwordx4 v108, s[64:65]\n\t" \
  "s_waitcnt lgkmcnt(2)\n\t" \
  "v_mfma_f32_32x32x16_bf16 v[32:47], v[136:139], v[128:131], v[32:47]\n\t" \
  "ds_read_b128 v[112:115], v166\n\t" \
  "ds_read_b128 v[116:119], v166 offset:4096\n\t" \
  "v_mfma_f32_32x32x16_bf16 v[48:63], v[136:139], v[132:135], v[48:63]\n\t" \
  "ds_read_b64_tr_b16 v[120:121], v173 offset:4096\n\t" \
  "ds_read_b64_tr_b16 v[122:123], v173 offset:4608\n\t" \
  "s_waitcnt lgkmcnt(4)\n\t" \
  "v_mfma_f32_32x32x16_bf16 v[64:79], v[140:143], v[128:131], v[64:79]\n\t" \
  "ds_read_b64_tr_b16 v[124:125], v173 offset:12288\n\t" \
  "ds_read_b64_tr_b16 v[126:127], v173 offset:12800\n\t" \
  "s_add_u32 m0, %[mx], 0\n\t" \
  "s_nop 0\n\t" \
  "global_load_lds_dwordx4 v104, s[60:61]\n\t" \
  "v_mfma_f32_32x32x16_bf16 v[80:95], v[140:143], v[132:135], v[80:95]\n\t" \
  "s_add_u32 m0, %[mx], 1024\n\t" \
  "s_nop 0\n\t" \
  "global_load_lds_dwordx4 v105, s[60:61]\n\t" \
  "s_waitcnt lgkmcnt(2)\n\t" \
  "v_mfma_f32_32x32x16_bf16 v[32:47], v[120:123], v[112:115], v[32:47]\n\t" \
  "ds_read_b128 v[128:131], v167\n\t" \
  "ds_read_b128 v[132:135], v167 offset:4096\n\t" \
  "v_mfma_f32_32x32x16_bf16 v[48:63], v[120:123], v[116:119], v[48:63]\n\t" \
  "ds_read_b64_tr_b16 v[136:137], v173 offset:6144\n\t" \
  "ds_read_b64_tr_b16 v[138:139], v173 offset:6656\n\t" \
  "s_waitcnt lgkmcnt(4)\n\t" \
  "v_mfma_f32_32x32x16_bf16 v[64:79], v[124:127], v[112:115], v[64:79]\n\t" \
  "ds_read_b64_tr_b16 v[140:141], v173 offset:14336\n\t" \
  "ds_read_b64_tr_b16 v[142:143], v173 offset:14848\n\t" \
  "s_add_u32 m0, %[mx], 2048\n\t" \
  "s_nop 0\n\t" \
  "global_load_lds_dwordx4 v106, s[60:61]\n\t" \
  "v_mfma_f32_32x32x16_bf16 v[80:95], v[124:127], v[116:119], v[80:95]\n\t" \
  "s_add_u32 m0, %[mx], 3072\n\t" \
  "s_nop 0\n\t" \
  "global_load_lds_dwordx4 v107, s[60:61]\n\t" \
  "s_waitcnt lgkmcnt(2)\n\t" \
  "v_mfma_f32_32x32x16_bf16 v[32:47], v[136:139], v[128:131], v[32:47]\n\t" \
  "v_mfma_f32_32x32x16_bf16 v[48:63], v[136:139], v[132:135], v[48:63]\n\t" \
  "s_waitcnt vmcnt(6) lgkmcnt(0)\n\t" \
  "s_barrier\n\t" \
  "ds_read_b128 v[112:115], v168\n\t" \
  "ds_read_b128 v[116:119], v168 offset:4096\n\t" \
  "ds_read_b64_tr_b16 v[120:121], v174 offset:0\n\t" \
  "ds_read_b64_tr_b16 v[122:123], v174 offset:512\n\t" \
  "v_mfma_f32_32x32x16_bf16 v[64:79], v[140:143], v[128:131], v[64:79]\n\t" \
  "ds_read_b64_tr_b16 v[124:125], v174 offset:8192\n\t" \
  "ds_read_b64_tr_b16 v[126:127], v174 offset:8704\n\t" \
  "v_mfma_f32_32x32x16_bf16 v[80:95], v[140:143], v[132:135], v[80:95]\n\t" \
  "s_add_u32 %[sk], %[sk], 1\n\t" \
  "s_cmp_eq_u32 %[sk], 10\n\t" \
  "s_cselect_b32 %[sk], 0, %[sk]\n\t" \
  "s_lshl_b32 %[st], %[sk], 7\n\t" \
  "s_add_u32 s60, s74, %[st]\n\t" \
  "s_addc_u32 s61, s75, 0\n\t" \
  "s_mul_i32 %[st], %[sk], %[wst]\n\t" \
  "s_add_u32 s62, s76, %[st]\n\t" \
  "s_addc_u32 s63, s77, 0\n\t" \
  "s_add_u32 s64, s78, %[st]\n\t" \
  "s_addc_u32 s65, s79, 0\n\t" \
  "s_waitcnt lgkmcnt(2)\n\t" \
  "v_mfma_f32_32x32x16_bf16 v[32:47], v[120:123], v[112:115], v[32:47]\n\t" \
  "ds_read_b128 v[128:131], v169\n\t" \
  "ds_read_b128 v[132:135], v169 offset:4096\n\t" \
  "v_mfma_f32_32x32x16_bf16 v[48:63], v[120:123], v[116:119], v[48:63]\n\t" \
  "ds_read_b64_tr_b16 v[136:137], v174 offset:2048\n\t" \
  "ds_read_b64_tr_b16 v[138:139], v174 offset:2560\n\t" \
  "s_waitcnt lgkmcnt(4)\n\t" \
  "v_mfma_f32_32x32x16_bf16 v[64:79], v[124:127], v[112:115], v[64:79]\n\t" \
  "ds_read_b64_tr_b16 v[140:141], v174 offset:10240\n\t" \
  "ds_read_b64_tr_b16 v[142:143], v174 offset:10752\n\t" \
  "ds_read_b128 v[176:179], v100\n\t" \
  "ds_read_b128 v[180:183], v100 offset:4096\n\t" \
  "s_add_u32 m0, %[mw], 52224\n\t" \
  "s_nop 0\n\t" \
  "global_load_lds_dwordx4 v108, s[62:63]\n\t" \
  "v_mfma_f32_32x32x16_bf16 v[80:95], v[124:127], v[116:119], v[80:95]\n\t" \
  "s_add_u32 m0, %[mw], 60416\n\t" \
  "s_nop 0\n\t" \
  "global_load_lds_dwordx4 v108, s[64:65]\n\t" \
  "s_waitcnt lgkmcnt(4)\n\t" \
  "v_mfma_f32_32x32x16_bf16 v[32:47], v[136:139], v[128:131], v[32:47]\n\t" \
  "ds_read_b128 v[112:115], v170\n\t" \
  "ds_read_b128 v[116:119], v170 offset:4096\n\t" \
  "v_mfma_f32_32x32x16_bf16 v[48:63], v[136:139], v[132:135], v[48:63]\n\t" \
  "ds_read_b64_tr_b16 v[120:121], v174 offset:4096\n\t" \
  "ds_read_b64_tr_b16 v[122:123], v174 offset:4608\n\t" \
  "s_waitcnt lgkmcnt(6)\n\t" \
  "v_mfma_f32_32x32x16_bf16 v[64:79], v[140:143], v[128:131], v[64:79]\n\t" \
  "ds_read_b64_tr_b16 v[124:125], v174 offset:12288\n\t" \
  "ds_read_b64_tr_b16 v[126:127], v174 offset:12800\n\t" \
  "ds_read_b128 v[184:187], v101\n\t" \
  "ds_read_b128 v[188:191], v101 offset:4096\n\t" \
  "s_add_u32 m0, %[mx], 52224\n\t" \
  "s_nop 0\n\t" \
  "global_load_lds_dwordx4 v104, s[60:61]\n\t" \
  "v_mfma_f32_32x32x16_bf16 v[80:95], v[140:143], v[132:135], v[80:95]\n\t" \
  "s_add_u32 m0, %[mx], 53248\n\t" \
  "s_nop 0\n\t" \
  "global_load_lds_dwordx4 v105, s[60:61]\n\t" \
  "s_waitcnt lgkmcnt(4)\n\t" \
  "v_mfma_f32_32x32x16_bf16 v[32:47], v[120:123], v[112:115], v[32:47]\n\t" \
  "ds_read_b128 v[128:131], v171\n\t" \
  "ds_read_b128 v[132:135], v171 offset:4096\n\t" \
  "v_mfma_f32_32x32x16_bf16 v[48:63], v[120:123], v[116:119], v[48:63]\n\t" \
  "ds_read_b64_tr_b16 v[136:137], v174 offset:6144\n\t" \
  "ds_read_b64_tr_b16 v[138:139], v174 offset:6656\n\t" \
  "s_waitcnt lgkmcnt(6)\n\t" \
  "v_mfma_f32_32x32x16_bf16 v[64:79], v[124:127], v[112:115], v[64:79]\n\t" \
  "ds_read_b64_tr_b16 v[140:141], v174 offset:14336\n\t" \
  "ds_read_b64_tr_b16 v[142:143], v174 offset:14848\n\t" \
  "s_add_u32 m0, %[mx], 54272\n\t" \
  "s_nop 0\n\t" \
  "global_load_lds_dwordx4 v106, s[60:61]\n\t" \
  "v_mfma_f32_32x32x16_bf16 v[80:95], v[124:127], v[116:119], v[80:95]\n\t" \
  "s_add_u32 m0, %[mx], 55296\n\t" \
  "s_nop 0\n\t" \
  "global_load_lds_dwordx4 v107, s[60:61]\n\t" \
  "buffer_load_dword v154, v102, %[srd], %[fwq] offen sc1\n\t" \
  "s_waitcnt lgkmcnt(2)\n\t" \
  "v_mfma_f32_32x32x16_bf16 v[32:47], v[136:139], v[128:131], v[32:47]\n\t" \
  "v_mfma_f32_32x32x16_bf16 v[48:63], v[136:139], v[132:135], v[48:63]\n\t" \
  "s_waitcnt vmcnt(7) lgkmcnt(0)\n\t" \
  "s_barrier\n\t" \
  "ds_read_b128 v[112:115], v160\n\t" \
  "ds_read_b128 v[116:119], v160 offset:4096\n\t" \
  "ds_read_b64_tr_b16 v[120:121], v172 offset:0\n\t" \
  "ds_read_b64_tr_b16 v[122:123], v172 offset:512\n\t" \
  "v_mfma_f32_32x32x16_bf16 v[64:79], v[140:143], v[128:131], v[64:79]\n\t" \
  "ds_read_b64_tr_b16 v[124:125], v172 offset:8192\n\t" \
  "ds_read_b64_tr_b16 v[126:127], v172 offset:8704\n\t" \
  "v_mfma_f32_32x32x16_bf16 v[80:95], v[140:143], v[132:135], v[80:95]\n\t" \
  "s_add_u32 %[sk], %[sk], 1\n\t" \
  "s_cmp_eq_u32 %[sk], 10\n\t" \
  "s_cselect_b32 %[sk], 0, %[sk]\n\t" \
  "s_lshl_b32 %[st], %[sk], 7\n\t" \
  "s_add_u32 s60, s74, %[st]\n\t" \
  "s_addc_u32 s61, s75, 0\n\t" \
  "s_mul_i32 %[st], %[sk], %[wst]\n\t" \
  "s_add_u32 s62, s76, %[st]\n\t" \
  "s_addc_u32 s63, s77, 0\n\t" \
  "s_add_u32 s64, s78, %[st]\n\t" \
  "s_addc_u32 s65, s79, 0\n\t" \
  "s_waitcnt lgkmcnt(2)\n\t" \
  "v_mfma_f32_32x32x16_bf16 v[32:47], v[120:123], v[112:115], v[32:47]\n\t" \
  "ds_read_b128 v[128:131], v161\n\t" \
  "ds_read_b128 v[132:135], v161 offset:4096\n\t" \
  "v_mfma_f32_32x32x16_bf16 v[48:63], v[120:123], v[116:119], v[48:63]\n\t" \
  "ds_read_b64_tr_b16 v[136:137], v172 offset:2048\n\t" \
  "ds_read_b64_tr_b16 v[138:139], v172 offset:2560\n\t" \
  "s_waitcnt lgkmcnt(4)\n\t" \
  "v_mfma_f32_32x32x16_bf16 v[64:79], v[124:127], v[112:115], v[64:79]\n\t" \
  "ds_read_b64_tr_b16 v[140:141], v172 offset:10240\n\t" \
  "ds_read_b64_tr_b16 v[142:143], v172 offset:10752\n\t" \
  "ds_read_b128 v[192:195], v96\n\t" \
  "ds_read_b128 v[196:199], v96 offset:4096\n\t" \
  "s_add_u32 m0, %[mw], 104448\n\t" \
  "s_nop 0\n\t" \
  "global_load_lds_dwordx4 v108, s[62:63]\n\t" \
  "v_mfma_f32_32x32x16_bf16 v[80:95], v[124:127], v[116:119], v[80:95]\n\t" \
  "s_add_u32 m0, %[mw], 112640\n\t" \
  "s_nop 0\n\t" \
  "global_load_lds_dwordx4 v108, s[64:65]\n\t" \
  "s_waitcnt lgkmcnt(4)\n\t" \
  "v_mfma_f32_32x32x16_bf16 v[32:47], v[136:139], v[128:131], v[32:47]\n\t" \
  "ds_read_b128 v[112:115], v162\n\t" \
  "ds_read_b128 v[116:119], v162 offset:4096\n\t" \
  "v_mfma_f32_32x32x16_bf16 v[48:63], v[136:139], v[132:135], v[48:63]\n\t" \
  "ds_read_b64_tr_b16 v[120:121], v172 offset:4096\n\t" \
  "ds_read_b64_tr_b16 v[122:123], v172 offset:4608\n\t" \
  "s_waitcnt lgkmcnt(6)\n\t" \
  "v_mfma_f32_32x32x16_bf16 v[64:79], v[140:143], v[128:131], v[64:79]\n\t" \
  "ds_read_b64_tr_b16 v[124:125], v172 offset:12288\n\t" \
  "ds_read_b64_tr_b16 v[126:127], v172 offset:12800\n\t" \
  "ds_read_b128 v[200:203], v97\n\t" \
  "ds_read_b128 v[204:207], v97 offset:4096\n\t" \
  "s_add_u32 m0, %[mx], 104448\n\t" \
  "s_nop 0\n\t" \
  "global_load_lds_dwordx4 v104, s[60:61]\n\t" \
  "v_mfma_f32_32x32x16_bf16 v[80:95], v[140:143], v[132:135], v[80:95]\n\t" \
  "s_add_u32 m0, %[mx], 105472\n\t" \
  "s_nop 0\n\t" \
  "global_load_lds_dwordx4 v105, s[60:61]\n\t" \
  "s_waitcnt lgkmcnt(4)\n\t" \
  "v_mfma_f32_32x32x16_bf16 v[32:47], v[120:123], v[112:115], v[32:47]\n\t" \
  "ds_read_b128 v[128:131], v163\n\t" \
  "ds_read_b128 v[132:135], v163 offset:4096\n\t" \
  "v_mfma_f32_32x32x16_bf16 v[48:63], v[120:123], v[116:119], v[48:63]\n\t" \
  "ds_read_b64_tr_b16 v[136:137], v172 offset:6144\n\t" \
  "ds_read_b64_tr_b16 v[138:139], v172 offset:6656\n\t" \
  "s_waitcnt lgkmcnt(6)\n\t" \
  "v_mfma_f32_32x32x16_bf16 v[64:79], v[124:127], v[112:115], v[64:79]\n\t" \
  "ds_read_b64_tr_b16 v[140:141], v172 offset:14336\n\t" \
  "ds_read_b64_tr_b16 v[142:143], v172 offset:14848\n\t" \
  "s_add_u32 m0, %[mx], 106496\n\t" \
  "s_nop 0\n\t" \
  "global_load_lds_dwordx4 v106, s[60:61]\n\t" \
  "v_mfma_f32_32x32x16_bf16 v[80:95], v[124:127], v[116:119], v[80:95]\n\t" \
  "s_add_u32 m0, %[mx], 107520\n\t" \
  "s_nop 0\n\t" \
  "global_load_lds_dwordx4 v107, s[60:61]\n\t" \
  "s_waitcnt lgkmcnt(2)\n\t" \
  "v_mfma_f32_32x32x16_bf16 v[32:47], v[136:139], v[128:131], v[32:47]\n\t" \
  "v_mfma_f32_32x32x16_bf16 v[48:63], v[136:139], v[132:135], v[48:63]\n\t" \
  "s_waitcnt vmcnt(7) lgkmcnt(0)\n\t" \
  "s_barrier\n\t" \
  "ds_read_b128 v[112:115], v164\n\t" \
  "ds_read_b128 v[116:119], v164 offset:4096\n\t" \
  "ds_read_b64_tr_b16 v[120:121], v173 offset:0\n\t" \
  "ds_read_b64_tr_b16 v[122:123], v173 offset:512\n\t" \
  "v_mfma_f32_32x32x16_bf16 v[64:79], v[140:143], v[128:131], v[64:79]\n\t" \
  "ds_read_b64_tr_b16 v[124:125], v173 offset:8192\n\t" \
  "ds_read_b64_tr_b16 v[126:127], v173 offset:8704\n\t" \
  "v_mfma_f32_32x32x16_bf16 v[80:95], v[140:143], v[132:135], v[80:95]\n\t" \
  "s_add_u32 %[sk], %[sk], 1\n\t" \
  "s_cmp_eq_u32 %[sk], 10\n\t" \
  "s_cselect_b32 %[sk], 0, %[sk]\n\t" \
  "s_lshl_b32 %[st], %[sk], 7\n\t" \
  "s_add_u32 s60, s74, %[st]\n\t" \
  "s_addc_u32 s61, s75, 0\n\t" \
  "s_mul_i32 %[st], %[sk], %[wst]\n\t" \
  "s_add_u32 s62, s76, %[st]\n\t" \
  "s_addc_u32 s63, s77, 0\n\t" \
  "s_add_u32 s64, s78, %[st]\n\t" \
  "s_addc_u32 s65, s79, 0\n\t" \
  "s_waitcnt lgkmcnt(2)\n\t" \
  "v_mfma_f32_32x32x16_bf16 v[32:47], v[120:123], v[112:115], v[32:47]\n\t" \
  "ds_read_b128 v[128:131], v165\n\t" \
  "ds_read_b128 v[132:135], v165 offset:4096\n\t" \
  "v_mfma_f32_32x32x16_bf16 v[48:63], v[120:123], v[116:119], v[48:63]\n\t" \
  "ds_read_b64_tr_b16 v[136:137], v173 offset:2048\n\t" \
  "ds_read_b64_tr_b16 v[138:139], v173 offset:2560\n\t" \
  "s_waitcnt lgkmcnt(4)\n\t" \
  "v_mfma_f32_32x32x16_bf16 v[64:79], v[124:127], v[112:115], v[64:79]\n\t" \
  "ds_read_b64_tr_b16 v[140:141], v173 offset:10240\n\t" \
  "ds_read_b64_tr_b16 v[142:143], v173 offset:10752\n\t" \
  "ds_read_b128 v[208:211], v98\n\t" \
  "ds_read_b128 v[212:215], v98 offset:4096\n\t" \
  "s_add_u32 m0, %[mw], 0\n\t" \
  "s_nop 0\n\t" \
  "global_load_lds_dwordx4 v108, s[62:63]\n\t" \
  "v_mfma_f32_32x32x16_bf16 v[80:95], v[124:127], v[116:119], v[80:95]\n\t" \
  "s_add_u32 m0, %[mw], 8192\n\t" \
  "s_nop 0\n\t" \
  "global_load_lds_dwordx4 v108, s[64:65]\n\t" \
  "s_waitcnt lgkmcnt(4)\n\t" \
  "v_mfma_f32_32x32x16_bf16 v[32:47], v[136:139], v[128:131], v[32:47]\n\t" \
  "ds_read_b128 v[112:115], v166\n\t" \
  "ds_read_b128 v[116:119], v166 offset:4096\n\t" \
  "v_mfma_f32_32x32x16_bf16 v[48:63], v[136:139], v[132:135], v[48:63]\n\t" \
  "ds_read_b64_tr_b16 v[120:121], v173 offset:4096\n\t" \
  "ds_read_b64_tr_b16 v[122:123], v173 offset:4608\n\t" \
  "s_waitcnt lgkmcnt(6)\n\t" \
  "v_mfma_f32_32x32x16_bf16 v[64:79], v[140:143], v[128:131], v[64:79]\n\t" \
  "ds_read_b64_tr_b16 v[124:125], v173 offset:12288\n\t" \
  "ds_read_b64_tr_b16 v[126:127], v173 offset:12800\n\t" \
  "ds_read_b128 v[216:219], v99\n\t" \
  "ds_read_b128 v[220:223], v99 offset:4096\n\t" \
  "s_add_u32 m0, %[mx], 0\n\t" \
  "s_nop 0\n\t" \
  "global_load_lds_dwordx4 v104, s[60:61]\n\t" \
  "v_mfma_f32_32x32x16_bf16 v[80:95], v[140:143], v[132:135], v[80:95]\n\t" \
  "s_add_u32 m0, %[mx], 1024\n\t" \
  "s_nop 0\n\t" \
  "global_load_lds_dwordx4 v105, s[60:61]\n\t" \
  "s_waitcnt lgkmcnt(4)\n\t" \
  "v_mfma_f32_32x32x16_bf16 v[32:47], v[120:123], v[112:115], v[32:47]\n\t" \
  "ds_read_b128 v[128:131], v167\n\t" \
  "ds_read_b128 v[132:135], v167 offset:4096\n\t" \
  "v_mfma_f32_32x32x16_bf16 v[48:63], v[120:123], v[116:119], v[48:63]\n\t" \
  "ds_read_b64_tr_b16 v[136:137], v173 offset:6144\n\t" \
  "ds_read_b64_tr_b16 v[138:139], v173 offset:6656\n\t" \
  "s_waitcnt lgkmcnt(6)\n\t" \
  "v_mfma_f32_32x32x16_bf16 v[64:79], v[124:127], v[112:115], v[64:79]\n\t" \
  "ds_read_b64_tr_b16 v[140:141], v173 offset:14336\n\t" \
  "ds_read_b64_tr_b16 v[142:143], v173 offset:14848\n\t" \
  "s_add_u32 m0, %[mx], 2048\n\t" \
  "s_nop 0\n\t" \
  "global_load_lds_dwordx4 v106, s[60:61]\n\t" \
  "v_mfma_f32_32x32x16_bf16 v[80:95], v[124:127], v[116:119], v[80:95]\n\t" \
  "s_add_u32 m0, %[mx], 3072\n\t" \
  "s_nop 0\n\t" \
  "global_load_lds_dwordx4 v107, s[60:61]\n\t" \
  "s_waitcnt lgkmcnt(2)\n\t" \
  "v_mfma_f32_32x32x16_bf16 v[32:47], v[136:139], v[128:131], v[32:47]\n\t" \
  "v_mfma_f32_32x32x16_bf16 v[48:63], v[136:139], v[132:135], v[48:63]\n\t" \
  "s_waitcnt vmcnt(6) lgkmcnt(0)\n\t" \
  "v_readfirstlane_b32 %[tag], v155\n\t" \
  "s_mov_b32 %[st], 0\n\t" \
  "s_add_u32 %[tag], %[tag], 1\n\t" \
  "s_nop 1\n\t" \
  "L_fq_%=:\n\t" \
  "v_cmp_ne_u32_e32 vcc, %[tag], v154\n\t" \
  "s_cmp_lt_u32 %[st], 0x100000\n\t" \
  "s_cselect_b64 %[tp], vcc, 0\n\t" \
  "s_cmp_lg_u64 %[tp], 0\n\t" \
  "s_cbranch_scc0 L_fqd_%=\n\t" \
  "buffer_load_dword v154, v102, %[srd], %[fwq] offen sc1\n\t" \
  "s_add_u32 %[st], %[st], 1\n\t" \
  "s_waitcnt vmcnt(0)\n\t" \
  "s_branch L_fq_%=\n\t" \
  "L_fqd_%=:\n\t" \
  "s_barrier\n\t" \
  "ds_read_b128 v[112:115], v168\n\t" \
  "ds_read_b128 v[116:119], v168 offset:4096\n\t" \
  "ds_read_b64_tr_b16 v[120:121], v174 offset:0\n\t" \
  "ds_read_b64_tr_b16 v[122:123], v174 offset:512\n\t" \
  "v_mfma_f32_32x32x16_bf16 v[64:79], v[140:143], v[128:131], v[64:79]\n\t" \
  "ds_read_b64_tr_b16 v[124:125], v174 offset:8192\n\t" \
  "ds_read_b64_tr_b16 v[126:127], v174 offset:8704\n\t" \
  "v_mfma_f32_32x32x16_bf16 v[80:95], v[140:143], v[132:135], v[80:95]\n\t" \
  "s_waitcnt lgkmcnt(2)\n\t" \
  "v_mfma_f32_32x32x16_bf16 v[32:47], v[120:123], v[112:115], v[32:47]\n\t" \
  "ds_read_b128 v[128:131], v169\n\t" \
  "ds_read_b128 v[132:135], v169 offset:4096\n\t" \
  "v_mfma_f32_32x32x16_bf16 v[48:63], v[120:123], v[116:119], v[48:63]\n\t" \
  "ds_read_b64_tr_b16 v[136:137], v174 offset:2048\n\t" \
  "ds_read_b64_tr_b16 v[138:139], v174 offset:2560\n\t" \
  "s_waitcnt lgkmcnt(4)\n\t" \
  "v_mfma_f32_32x32x16_bf16 v[64:79], v[124:127], v[112:115], v[64:79]\n\t" \
  "ds_read_b64_tr_b16 v[140:141], v174 offset:10240\n\t" \
  "ds_read_b64_tr_b16 v[142:143], v174 offset:10752\n\t" \
  "ds_read_b128 v[224:227], v100\n\t" \
  "ds_read_b128 v[228:231], v100 offset:4096\n\t" \
  "s_add_u32 m0, %[mq], 52224\n\t" \
  "s_nop 0\n\t" \
  "global_load_lds_dwordx4 v144, s[68:69]\n\t" \
  "v_mfma_f32_32x32x16_bf16 v[80:95], v[124:127], v[116:119], v[80:95]\n\t" \
  "s_add_u32 m0, %[mq], 60416\n\t" \
  "s_nop 0\n\t" \
  "global_load_lds_dwordx4 v145, s[68:69]\n\t" \
  "s_waitcnt lgkmcnt(4)\n\t" \
  "v_mfma_f32_32x32x16_bf16 v[32:47], v[136:139], v[128:131], v[32:47]\n\t" \
  "ds_read_b128 v[112:115], v170\n\t" \
  "ds_read_b128 v[116:119], v170 offset:4096\n\t" \
  "v_mfma_f32_32x32x16_bf16 v[48:63], v[136:139], v[132:135], v[48:63]\n\t" \
  "ds_read_b64_tr_b16 v[120:121], v174 offset:4096\n\t" \
  "ds_read_b64_tr_b16 v[122:123], v174 offset:4608\n\t" \
  "s_waitcnt lgkmcnt(6)\n\t" \
  "v_mfma_f32_32x32x16_bf16 v[64:79], v[140:143], v[128:131], v[64:79]\n\t" \
  "ds_read_b64_tr_b16 v[124:125], v174 offset:12288\n\t" \
  "ds_read_b64_tr_b16 v[126:127], v174 offset:12800\n\t" \
  "ds_read_b128 v[232:235], v101\n\t" \
  "ds_read_b128 v[236:239], v101 offset:4096\n\t" \
  "s_cmp_lt_u32 %[wid], 5\n\t" \
  "s_cbranch_scc0 L_qw8_%=\n\t" \
  "s_add_u32 m0, %[mq], 68608\n\t" \
  "s_nop 0\n\t" \
  "global_load_lds_dwordx4 v146, s[68:69]\n\t" \
  "s_branch L_qe8_%=\n\t" \
  "L_qw8_%=:\n\t" \
  "s_add_u32 m0, %[mq], 68608\n\t" \
  "s_sub_u32 %[st], %[sw3], 8192\n\t" \
  "buffer_load_dwordx4 v103, %[srd], %[st] offen sc1 lds\n\t" \
  "L_qe8_%=:\n\t" \
  "v_mfma_f32_32x32x16_bf16 v[80:95], v[140:143], v[132:135], v[80:95]\n\t" \
  "s_add_u32 m0, %[mq], 76800\n\t" \
  "s_add_u32 %[st], %[sw3], 0\n\t" \
  "buffer_load_dwordx4 v103, %[srd], %[st] offen sc1 lds\n\t" \
  "s_waitcnt lgkmcnt(4)\n\t" \
  "v_mfma_f32_32x32x16_bf16 v[32:47], v[120:123], v[112:115], v[32:47]\n\t" \
  "ds_read_b128 v[128:131], v171\n\t" \
  "ds_read_b128 v[132:135], v171 offset:4096\n\t" \
  "v_mfma_f32_32x32x16_bf16 v[48:63], v[120:123], v[116:119], v[48:63]\n\t" \
  "ds_read_b64_tr_b16 v[136:137], v174 offset:6144\n\t" \
  "ds_read_b64_tr_b16 v[138:139], v174 offset:6656\n\t" \
  "s_waitcnt lgkmcnt(6)\n\t" \
  "v_mfma_f32_32x32x16_bf16 v[64:79], v[124:127], v[112:115], v[64:79]\n\t" \
  "ds_read_b64_tr_b16 v[140:141], v174 offset:14336\n\t" \
  "ds_read_b64_tr_b16 v[142:143], v174 offset:14848\n\t" \
  "s_add_u32 m0, %[mq], 84992\n\t" \
  "s_add_u32 %[st], %[sw3], 8192\n\t" \
  "buffer_load_dwordx4 v103, %[srd], %[st] offen sc1 lds\n\t" \
  "v_mfma_f32_32x32x16_bf16 v[80:95], v[124:127], v[116:119], v[80:95]\n\t" \
  "s_add_u32 m0, %[mq], 93184\n\t" \
  "s_add_u32 %[st], %[sw3], 16384\n\t" \
  "buffer_load_dwordx4 v103, %[srd], %[st] offen sc1 lds\n\t" \
  "s_waitcnt lgkmcnt(2)\n\t" \
  "v_mfma_f32_32x32x16_bf16 v[32:47], v[136:139], v[128:131], v[32:47]\n\t" \
  "v_mfma_f32_32x32x16_bf16 v[48:63], v[136:139], v[132:135], v[48:63]\n\t" \
  "s_waitcnt vmcnt(6) lgkmcnt(0)\n\t" \
  "s_barrier\n\t" \
  "ds_read_b128 v[112:115], v160\n\t" \
  "ds_read_b128 v[116:119], v160 offset:4096\n\t" \
  "ds_read_b64_tr_b16 v[120:121], v172 offset:0\n\t" \
  "ds_read_b64_tr_b16 v[122:123], v172 offset:512\n\t" \
  "v_mfma_f32_32x32x16_bf16 v[64:79], v[140:143], v[128:131], v[64:79]\n\t" \
  "ds_read_b64_tr_b16 v[124:125], v172 offset:8192\n\t" \
  "ds_read_b64_tr_b16 v[126:127], v172 offset:8704\n\t" \
  "v_mfma_f32_32x32x16_bf16 v[80:95], v[140:143], v[132:135], v[80:95]\n\t" \
  "s_waitcnt lgkmcnt(2)\n\t" \
  "v_mfma_f32_32x32x16_bf16 v[32:47], v[120:123], v[112:115], v[32:47]\n\t" \
  "s_add_u32 %[st], %[wrb], 0\n\t" \
  "buffer_load_dwordx4 v[104:107], v175, %[srd], %[st] offen sc1\n\t" \
  "s_add_u32 %[st], %[wrb], 5120\n\t" \
  "buffer_load_dwordx4 v[108:111], v175, %[srd], %[st] offen sc1\n\t" \
  "s_add_u32 %[st], %[wrb], 10240\n\t" \
  "buffer_load_dwordx4 v[152:155], v175, %[srd], %[st] offen sc1\n\t" \
  "s_add_u32 %[st], %[wrb], 15360\n\t" \
  "buffer_load_dwordx4 v[156:159], v175, %[srd], %[st] offen sc1\n\t" \
  "s_add_u32 %[st], %[wrb], 20480\n\t" \
  "buffer_load_dwordx4 v[164:167], v175, %[srd], %[st] offen sc1\n\t" \
  "s_add_u32 %[st], %[wrb], 25600\n\t" \
  "buffer_load_dwordx4 v[168:171], v175, %[srd], %[st] offen sc1\n\t" \
  "ds_read_b128 v[128:131], v161\n\t" \
  "ds_read_b128 v[132:135], v161 offset:4096\n\t" \
  "v_mfma_f32_32x32x16_bf16 v[48:63], v[120:123], v[116:119], v[48:63]\n\t" \
  "ds_read_b64_tr_b16 v[136:137], v172 offset:2048\n\t" \
  "ds_read_b64_tr_b16 v[138:139], v172 offset:2560\n\t" \
  "s_waitcnt lgkmcnt(4)\n\t" \
  "v_mfma_f32_32x32x16_bf16 v[64:79], v[124:127], v[112:115], v[64:79]\n\t" \
  "ds_read_b64_tr_b16 v[140:141], v172 offset:10240\n\t" \
  "ds_read_b64_tr_b16 v[142:143], v172 offset:10752\n\t" \
  "ds_read_b128 v[240:243], v96\n\t" \
  "ds_read_b128 v[244:247], v96 offset:4096\n\t" \
  "s_add_u32 m0, %[mq], 104448\n\t" \
  "s_nop 0\n\t" \
  "global_load_lds_dwordx4 v147, s[68:69]\n\t" \
  "v_mfma_f32_32x32x16_bf16 v[80:95], v[124:127], v[116:119], v[80:95]\n\t" \
  "s_add_u32 m0, %[mq], 112640\n\t" \
  "s_nop 0\n\t" \
  "global_load_lds_dwordx4 v148, s[68:69]\n\t" \
  "s_waitcnt lgkmcnt(4)\n\t" \
  "v_mfma_f32_32x32x16_bf16 v[32:47], v[136:139], v[128:131], v[32:47]\n\t" \
  "ds_read_b128 v[112:115], v162\n\t" \
  "ds_read_b128 v[116:119], v162 offset:4096\n\t" \
  "v_mfma_f32_32x32x16_bf16 v[48:63], v[136:139], v[132:135], v[48:63]\n\t" \
  "ds_read_b64_tr_b16 v[120:121], v172 offset:4096\n\t" \
  "ds_read_b64_tr_b16 v[122:123], v172 offset:4608\n\t" \
  "s_waitcnt lgkmcnt(6)\n\t" \
  "v_mfma_f32_32x32x16_bf16 v[64:79], v[140:143], v[128:131], v[64:79]\n\t" \
  "ds_read_b64_tr_b16 v[124:125], v172 offset:12288\n\t" \
  "ds_read_b64_tr_b16 v[126:127], v172 offset:12800\n\t" \
  "ds_read_b128 v[248:251], v97\n\t" \
  "ds_read_b128 v[252:255], v97 offset:4096\n\t" \
  "s_cmp_lt_u32 %[wid], 5\n\t" \
  "s_cbranch_scc0 L_qw9_%=\n\t" \
  "s_add_u32 m0, %[mq], 120832\n\t" \
  "s_nop 0\n\t" \
  "global_load_lds_dwordx4 v149, s[68:69]\n\t" \
  "s_branch L_qe9_%=\n\t" \
  "L_qw9_%=:\n\t" \
  "s_add_u32 m0, %[mq], 120832\n\t" \
  "s_add_u32 %[st], %[sw3], 17408\n\t" \
  "buffer_load_dwordx4 v103, %[srd], %[st] offen sc1 lds\n\t" \
  "L_qe9_%=:\n\t" \
  "v_mfma_f32_32x32x16_bf16 v[80:95], v[140:143], v[132:135], v[80:95]\n\t" \
  "s_add_u32 m0, %[mq], 129024\n\t" \
  "s_add_u32 %[st], %[sw3], 25600\n\t" \
  "buffer_load_dwordx4 v103, %[srd], %[st] offen sc1 lds\n\t" \
  "s_waitcnt lgkmcnt(4)\n\t" \
  "v_mfma_f32_32x32x16_bf16 v[32:47], v[120:123], v[112:115], v[32:47]\n\t" \
  "ds_read_b128 v[128:131], v163\n\t" \
  "ds_read_b128 v[132:135], v163 offset:4096\n\t" \
  "v_mfma_f32_32x32x16_bf16 v[48:63], v[120:123], v[116:119], v[48:63]\n\t" \
  "ds_read_b64_tr_b16 v[136:137], v172 offset:6144\n\t" \
  "ds_read_b64_tr_b16 v[138:139], v172 offset:6656\n\t" \
  "s_waitcnt lgkmcnt(6)\n\t" \
  "v_mfma_f32_32x32x16_bf16 v[64:79], v[124:127], v[112:115], v[64:79]\n\t" \
  "ds_read_b64_tr_b16 v[140:141], v172 offset:14336\n\t" \
  "ds_read_b64_tr_b16 v[142:143], v172 offset:14848\n\t" \
  "s_add_u32 m0, %[mq], 137216\n\t" \
  "s_add_u32 %[st], %[sw3], 33792\n\t" \
  "buffer_load_dwordx4 v103, %[srd], %[st] offen sc1 lds\n\t" \
  "v_mfma_f32_32x32x16_bf16 v[80:95], v[124:127], v[116:119], v[80:95]\n\t" \
  "s_add_u32 m0, %[mq], 145408\n\t" \
  "s_add_u32 %[st], %[sw3], 41984\n\t" \
  "buffer_load_dwordx4 v103, %[srd], %[st] offen sc1 lds\n\t" \
  "s_waitcnt lgkmcnt(2)\n\t" \
  "v_mfma_f32_32x32x16_bf16 v[32:47], v[136:139], v[128:131], v[32:47]\n\t" \
  "v_mfma_f32_32x32x16_bf16 v[48:63], v[136:139], v[132:135], v[48:63]\n\t" \
  "s_waitcnt lgkmcnt(0)\n\t" \
  "v_mfma_f32_32x32x16_bf16 v[64:79], v[140:143], v[128:131], v[64:79]\n\t" \
  "v_mfma_f32_32x32x16_bf16 v[80:95], v[140:143], v[132:135], v[80:95]\n\t" \
  "s_waitcnt vmcnt(6) lgkmcnt(0)\n\t" \
  "s_nop 7\n\t" \
  "s_nop 7\n\t" \
  ""
#define GEMM_CLOBBERS "v112", "v113", "v114", "v115", "v116", "v117", "v118", "v119", "v120", "v121", "v122", "v123", "v124", "v125", "v126", "v127", "v128", "v129", "v130", "v131", "v132", "v133", "v134", "v135", "v136", "v137", "v138", "v139", "v140", "v141", "v142", "v143", "v150", "v151", "s60", "s61", "s62", "s63", "s64", "s65", "s66", "s67", "s68", "s69", "s70", "s71", "s72", "s73", "s74", "s75", "s76", "s77", "s78", "s79"

constexpr unsigned QS_W = 21 * 1024, QS_A = K1_STAGE, QS_B = 2 * K1_STAGE;
struct QStage { unsigned voff[3]; const char* qx; unsigned wq0; };
DI void qstage_init(QStage& q, const bf16_t* __restrict__ x, int qb, int hg, unsigned lane, unsigned wid) {
#pragma unroll
  for (int i = 0; i < 3; ++i) {
    unsigned p = wid + 8 * i; if (p > 20) p = 20;
    const unsigned P = p * 64 + lane, row = P / 21, c = P - row * 21;
    q.voff[i] = row * (D * 2) + (c < 20 ? c : 19) * 16;
  }
  q.qx = (const char*)(x + (size_t)qb * 64 * D); q.wq0 = WS_WQ + hg * 80 * 2560;
}
DI void qstage_piece(const QStage& q, rsrc_t wsr, int quarter, unsigned region, unsigned lane, unsigned wid, int i) {
  unsigned p = wid + 8 * i; if (p >= 46) p -= 8;
  if (i < 2 || (i == 2 && wid < 5)) dma16(q.qx + quarter * 320, q.voff[i < 3 ? i : 2], region + p * 1024);
  else dma16b(wsr, lane * 16, q.wq0 + quarter * 25600 + (p - 21) * 1024, region + p * 1024);
}
DI void qstage_issue(const QStage& q, rsrc_t wsr, int quarter, unsigned region, unsigned lane, unsigned wid) {
#pragma unroll
  for (int i = 0; i < 6; ++i) qstage_piece(q, wsr, quarter, region, lane, wid, i);
}
struct QFrag { bf16x8 w[5], x[5][2]; };
DI unsigned qfrag_ax(unsigned region, unsigned lane, unsigned par) { return region + (lane & 31) * 336 + (lane >> 5) * 16 + par * 32; }
DI unsigned qfrag_aw(unsigned region, unsigned lane, unsigned rg, unsigned par) { const unsigned l31 = lane & 31; return region + QS_W + (par * 4 + rg) * 640 + ((lane >> 5) * 20 + (l31 < 20 ? l31 : 19)) * 16; }
DI void qfrag_read(QFrag& f, unsigned region, unsigned lane, unsigned rg, unsigned par) {
  const unsigned ax = qfrag_ax(region, lane, par), aw = qfrag_aw(region, lane, rg, par);
#pragma unroll
  for (int k = 0; k < 5; ++k) {
    f.w[k] = lds_b128(aw + k * 5120);
    f.x[k][0] = lds_b128(ax + k * 64); f.x[k][1] = lds_b128(ax + 32 * 336 + k * 64);
  }
}
DI void qfrag_mma(f32x16 (&acc)[2], const QFrag& f, int k) { acc[0] = mfma32(f.w[k], f.x[k][0], acc[0]); acc[1] = mfma32(f.w[k], f.x[k][1], acc[1]); }
DI void qgemm_quarter(f32x16 (&acc)[2], unsigned region, unsigned lane, unsigned rg, unsigned par) {
  QFrag f; qfrag_read(f, region, lane, rg, par);
#pragma unroll
  for (int k = 0; k < 5; ++k) qfrag_mma(acc, f, k);
}
DI unsigned mark_off(int qb, int hg, unsigned wid, int i) { unsigned f = wid + 8 * i; if (f >= 10) f -= 8; return WS_OF + ((qb * 2 + f / 5) * 40 + hg * 5 + f % 5) * 1024; }

#define KROT(x) ((((x) >> 1) * 5 / 2 + ((x) & 1) * 5) % 10)
struct XQ { f32x16 v[5]; bf16x8 w[6]; };
DI unsigned ffn_main(AOut& o, XQ& xq, int qb, int hg, rsrc_t wsr, const bf16_t* __restrict__ x, const bf16_t* __restrict__ wlo, const bf16_t* __restrict__ whi,
                     int rb, unsigned wid, unsigned shx, unsigned shw, unsigned k0) {
  const unsigned lane = fresh_lane();
  const unsigned wm = wid >> 1, wn = wid & 1, hh = lane >> 5;
  constexpr int ldw = F;
  unsigned vx[4];
  const unsigned px = (wid + shx) & 7, pw = (wid + 2 * shw) & 7;
#pragma unroll
  for (int i = 0; i < 4; ++i) { unsigned row = (px * 4 + i) * 8 + (lane >> 3), slot = lane & 7; vx[i] = row * (D * 2) + ((slot ^ ((row >> 1) & 7)) << 4); }
  unsigned vw;
  { unsigned kr = pw * 8 + (lane >> 3), slot = lane & 7, b = (slot >> 2) ^ ((kr >> 1) & 1); vw = kr * (ldw * 2) + (b * 4 + (slot & 3)) * 16; }
  const char* xb = (const char*)(x + (size_t)rb * 256 * D);
#pragma unroll
  for (int s = 0; s < 2; ++s) {
    const unsigned sk = k0 + s < 10 ? k0 + s : k0 + s - 10;
    dma16((const char*)wlo + (size_t)sk * 64 * ldw * 2, vw, s * K1_STAGE + K1_WL + pw * 1024);
    dma16((const char*)whi + (size_t)sk * 64 * ldw * 2, vw, s * K1_STAGE + K1_WH + pw * 1024);
#pragma unroll
    for (int i = 0; i < 4; ++i) dma16(xb + sk * 128, vx[i], s * K1_STAGE + K1_X + (px * 4 + i) * 1024);
  }
  SB();
  unsigned tag;
  {
    const unsigned l31 = lane & 31, q4 = (lane & 15) >> 2, p4 = lane & 3, b16 = (lane >> 4) & 1, par = wid >> 2;
    unsigned ax[3][4], aw[3], axq[3][2], vq[2][3];
#pragma unroll
    for (int sl = 0; sl < 3; ++sl) {
#pragma unroll
      for (int kk = 0; kk < 4; ++kk) ax[sl][kk] = sl * K1_STAGE + K1_X + (wm * 64 + l31) * 128 + (((2 * kk + hh) ^ ((l31 >> 1) & 7)) << 4);
      aw[sl] = sl * K1_STAGE + K1_WL + (8 * hh + q4) * 128 + ((wn ^ ((q4 >> 1) & 1)) * 64) + 32 * b16 + 8 * p4;
#pragma unroll
      for (int st = 0; st < 2; ++st) axq[sl][st] = sl * K1_STAGE + K1_X + ((qb & 3) * 64 + l31) * 128 + (((2 * (par + 2 * st) + hh) ^ ((l31 >> 1) & 7)) << 4);
    }
#pragma unroll
    for (int i = 0; i < 3; ++i) {
      unsigned p = wid + 8 * i; if (p > 20) p = 20;
      const unsigned P = p * 64 + lane, row = P / 21, c0 = P - row * 21, c = c0 < 20 ? c0 : 19;
#pragma unroll
      for (int j = 0; j < 2; ++j) { unsigned ch = 8 * k0 + 20 * j + c; if (ch >= 80) ch -= 80; vq[j][i] = row * (D * 2) + ch * 16; }
    }
    const char* xin = xb; const char* wlin = (const char*)wlo; const char* whin = (const char*)whi;
    const char* qxin = (const char*)(x + (size_t)qb * 64 * D);
    unsigned sk = rfl(k0 + 2 < 10 ? k0 + 2 : k0 + 2 - 10), st; unsigned long long tp;
    const unsigned wst = 64 * ldw * 2, mx = rfl(K1_X + px * 4096), mw = rfl(K1_WL + pw * 1024), mq = rfl(wid * 1024);
    const unsigned sw3 = rfl(WS_WQ + (hg * 80 + 4 * k0) * 2560 + (wid + 3) * 1024);
    const unsigned vfo = (wid * 5 + (lane < 5 ? lane : 4)) * 128, l16 = lane * 16, vwr = (hh * 20 + (l31 < 20 ? l31 : 19)) * 16;
    const unsigned wrb = rfl(WS_WQ + ((hg * 80 + 4 * (k0 + 5) + par) * 4 + (wid & 3)) * 640);
    const unsigned mk0 = rfl(mark_off(qb, hg, wid, 0)), mk1 = rfl(mark_off(qb, hg, wid, 1)), fwq = WS_FWQ, epo = WS_EPOCH;
    WAITV(6);
    BARRIER();
    asm volatile(GEMM_PLAIN
      : "={v[32:47]}"(o.acc[0][0]), "={v[48:63]}"(o.acc[0][1]), "={v[64:79]}"(o.acc[1][0]), "={v[80:95]}"(o.acc[1][1]),
        "={v[176:191]}"(xq.v[0]), "={v[192:207]}"(xq.v[1]), "={v[208:223]}"(xq.v[2]), "={v[224:239]}"(xq.v[3]), "={v[240:255]}"(xq.v[4]),
        "={v[104:107]}"(xq.w[0]), "={v[108:111]}"(xq.w[1]), "={v[152:155]}"(xq.w[2]), "={v[156:159]}"(xq.w[3]), "={v[164:167]}"(xq.w[4]), "={v[168:171]}"(xq.w[5]),
        [sk] "+s"(sk), [st] "=&s"(st), [tp] "=&s"(tp), [tag] "=&s"(tag)
      : "{v160}"(ax[0][0]), "{v161}"(ax[0][1]), "{v162}"(ax[0][2]), "{v163}"(ax[0][3]), "{v164}"(ax[1][0]), "{v165}"(ax[1][1]), "{v166}"(ax[1][2]), "{v167}"(ax[1][3]),
        "{v168}"(ax[2][0]), "{v169}"(ax[2][1]), "{v170}"(ax[2][2]), "{v171}"(ax[2][3]), "{v172}"(aw[0]), "{v173}"(aw[1]), "{v174}"(aw[2]),
        "{v96}"(axq[0][0]), "{v97}"(axq[0][1]), "{v98}"(axq[1][0]), "{v99}"(axq[1][1]), "{v100}"(axq[2][0]), "{v101}"(axq[2][1]), "{v102}"(vfo), "{v103}"(l16),
        "{v104}"(vx[0]), "{v105}"(vx[1]), "{v106}"(vx[2]), "{v107}"(vx[3]), "{v108}"(vw),
        "{v144}"(vq[0][0]), "{v145}"(vq[0][1]), "{v146}"(vq[0][2]), "{v147}"(vq[1][0]), "{v148}"(vq[1][1]), "{v149}"(vq[1][2]), "{v175}"(vwr),
        [xin_] "s"(xin), [wlin_] "s"(wlin), [whin_] "s"(whin), [wst] "s"(wst), [mx] "s"(mx), [mw] "s"(mw), [mq] "s"(mq),
        [qxin] "s"(qxin), [sw3] "s"(sw3), [wrb] "s"(wrb), [srd] "s"(wsr), [wid] "s"(wid), [epo] "s"(epo), [mk0] "s"(mk0), [mk1] "s"(mk1), [fwq] "s"(fwq)
      : GEMM_CLOBBERS, "vcc", "scc", "memory");
  }
  return tag;
}

DI void pack_issue(const bf16_t* __restrict__ wd, const bf16_t* __restrict__ wo, int item, unsigned PK_LDS, unsigned lane, unsigned wid) {
  const int pr = (item - 108) % 5, phg = (item - 108) / 5;
  const char* src = item < 108 ? (const char*)(wd + (size_t)item * 16 * D) : (const char*)(wo + (size_t)(phg * 80 + (pr < 4 ? pr * 20 : 16)) * D);
  const bool seg = item >= 108 && pr == 4;
#pragma unroll
  for (int i = 0; i < 3; ++i) {
    unsigned p = wid + 8 * i; if (p >= 20) p -= 8;
    unsigned of = p * 1024 + lane * 16; if (seg) of += (of / 5120) * (20 * 1280 - 5120);
    dma16(src, of, PK_LDS + p * 1024);
  }
}
DI void pack_store(rsrc_t wsr, int item, unsigned PK_LDS, unsigned lane, unsigned wid) {
  const unsigned hh = lane >> 5, q4 = (lane & 15) >> 2, p4 = lane & 3, b16 = (lane >> 4) & 1;
  const bool perm = item >= 108 && (item - 108) % 5 < 4;
  const unsigned r0 = perm ? 4 * hh + q4 : 8 * hh + q4, r1 = perm ? 8 + 4 * hh + q4 : 8 * hh + 4 + q4;
#pragma unroll
  for (int u = 0; u < 3; ++u) {
    const unsigned cbyte = (wid * 80 + u * 32 + 16 * b16 + 4 * p4) * 2;
    bf16x8 f = cat4(lds_tr(PK_LDS + r0 * 1280 + cbyte), lds_tr(PK_LDS + r1 * 1280 + cbyte));
    bst128(__builtin_bit_cast(u32x4, f), wsr, lane * 16, WS_WP + ((wid * 148 + item) * 3 + u) * 1024);
  }
}

DI void packq_store(rsrc_t wsr, int ks, unsigned base, unsigned lane, unsigned wid) {
  const unsigned l31 = lane & 31, hh = lane >> 5, q4 = (lane & 15) >> 2, p4 = lane & 3, b16 = (lane >> 4) & 1;
#pragma unroll
  for (int h = 0; h < 4; ++h) {
    const unsigned cbyte = (wid * 80 + h * 20 + 16 * b16 + 4 * p4) * 2;
    bf16x8 f = cat4(lds_tr(base + (8 * hh + q4) * 1280 + cbyte), lds_tr(base + (8 * hh + 4 + q4) * 1280 + cbyte));
    if (l31 < 20) { bst128(__builtin_bit_cast(u32x4, f), wsr, (hh * 20 + l31) * 16, WS_WQ + ((wid * 80 + ks) * 4 + h) * 640); bst128(__builtin_bit_cast(u32x4, f), wsr, (hh * 20 + l31) * 16, WS_WQ + ((wid * 80 + 40 + ks) * 4 + h) * 640); }
  }
}

DI unsigned gelu_pair(const AOut& o, int j, int r) {
  constexpr float C0 = -2.0f * 1.4426950408889634f * 0.7978845608028654f, C1 = C0 * 0.044715f;
  const f32x2 g = {o.acc[0][j][r], o.acc[0][j][r + 1]}, u = {o.acc[1][j][r], o.acc[1][j][r + 1]};
  const f32x2 z = g * (g * g * C1 + C0);
  const f32x2 sg = {__builtin_amdgcn_rcpf(1.0f + __builtin_amdgcn_exp2f(z[0])), __builtin_amdgcn_rcpf(1.0f + __builtin_amdgcn_exp2f(z[1]))};
  const f32x2 v = g * sg * u;
  return pk2(v[0], v[1]);
}
DI void h_store(const unsigned (&pk)[8], int j, rsrc_t wsr, unsigned lane, unsigned rt, unsigned ks0) {
#pragma unroll
  for (int g = 0; g < 4; g += 2) {
    auto r0 = __builtin_amdgcn_permlane32_swap(pk[2 * g], pk[2 * g + 2], false, false);
    auto r1 = __builtin_amdgcn_permlane32_swap(pk[2 * g + 1], pk[2 * g + 3], false, false);
    u32x4 v = {r0[0], r1[0], r0[1], r1[1]};
    bst128(v, wsr, lane * 16, WS_HF + ((rt + j) * 108 + ks0 + (g >> 1)) * 1024);
  }
}
struct BPre { bf16x8 qa[2]; bf16x4 qlo[2]; float km, gq, gf; };
template <bool LEAD>
DI void q_finish(BPre& p, f32x16 (&qacc)[2], unsigned xbase, unsigned lane, unsigned wid) {
  const unsigned rg = wid & 3, kh = wid >> 2;
  if (LEAD) BARRIER();
#pragma unroll
  for (int j = 0; j < 2; ++j)
#pragma unroll
    for (int g = 0; g < 3; ++g)
      *(LDSP f32x4*)(size_t)(xbase + (((rg * 2 + kh) * 2 + j) * 3 + g) * 1024 + lane * 16) = f32x4{qacc[j][4 * g], qacc[j][4 * g + 1], qacc[j][4 * g + 2], qacc[j][4 * g + 3]};
  BARRIER();
#pragma unroll
  for (int j = 0; j < 2; ++j) {
    unsigned pk[6];
#pragma unroll
    for (int g = 0; g < 3; ++g) {
      const f32x4 o = *(LDSP const f32x4*)(size_t)(xbase + (((rg * 2 + (kh ^ 1)) * 2 + j) * 3 + g) * 1024 + lane * 16);
      pk[2 * g] = pk2((qacc[j][4 * g] + o[0]) * QSCALE, (qacc[j][4 * g + 1] + o[1]) * QSCALE);
      pk[2 * g + 1] = pk2((qacc[j][4 * g + 2] + o[2]) * QSCALE, (qacc[j][4 * g + 3] + o[3]) * QSCALE);
    }
    p.qa[j] = __builtin_bit_cast(bf16x8, u32x4{pk[0], pk[1], pk[2], pk[3]});
    p.qlo[j] = __builtin_bit_cast(bf16x4, u32x2{pk[4], pk[5]});
  }
}

constexpr int K2_SLOT = 2 * KV_CHUNK, K2_HALF = KV_CHUNK, MEGA_LDS = 163840;
constexpr unsigned RB = 1024, P0 = RB + 2 * K2_SLOT;
constexpr unsigned QS_C = 0;

DI void mark_of(rsrc_t wsr, int qb, int hg, unsigned lane, unsigned wid) {
#pragma unroll
  for (int i = 0; i < 2; ++i) {
    unsigned f = wid + 8 * i; if (f >= 10) f -= 8;
    bst128(u32x4{~0u, ~0u, ~0u, ~0u}, wsr, lane * 16, WS_OF + ((qb * 2 + f / 5) * 40 + hg * 5 + f % 5) * 1024);
  }
}
DI void kv_piece(rsrc_t wsr, unsigned lane, unsigned wid, int i) {
  const unsigned rg = wid & 3, kh = wid >> 2, pc = rg + 4 * i;
  dma16b(wsr, lane * 16, WS_KV + (kh * 4) * KV_CHUNK + pc * 1024, P0 + kh * K2_HALF + pc * 1024);
}
DI void kv_scalars(BPre& p, rsrc_t wsr, int qb, unsigned lane, unsigned wid) {
  const unsigned kh = wid >> 2, l31 = lane & 31;
  p.km = bld32f(wsr, lane * 4, WS_KM);
  p.gq = bld32f(wsr, l31 * 4, WS_G + (qb * 64 + kh * 32) * 4);
  p.gf = bld32f(wsr, l31 * 4, WS_G + (qb * 64 + (wid & 1) * 32) * 4);
}
constexpr unsigned KV_WK = 4 * QS_W, KV_WV = KV_WK + 4 * 7168, KV_GWO = 6400, KV_RED = 143360;
DI unsigned kv_main(const bf16_t* __restrict__ x, const bf16_t* __restrict__ wq, const bf16_t* __restrict__ wk, const bf16_t* __restrict__ wv, const bf16_t* __restrict__ gate_w,
                    const bf16_t* __restrict__ wd, const bf16_t* __restrict__ wo, int i40, int qb, int hg, int rep, BPre& bp, unsigned wid, char* __restrict__ ws) {
  const unsigned lane = fresh_lane(), l31 = lane & 31, hh = lane >> 5, q4 = (lane & 15) >> 2, p4 = lane & 3, b16 = (lane >> 4) & 1;
  const rsrc_t wsr = make_rsrc(ws, WS_END);
  const bool kv = i40 < 32;
  const char* xr = (const char*)(x + (size_t)(kv ? i40 : 0) * 64 * D);
  unsigned ep = __builtin_amdgcn_raw_buffer_load_b32(wsr, 0, WS_EPOCH, SC1);
#pragma unroll
  for (int i = 0; i < 3; ++i) { unsigned p = wid + 8 * i; if (p >= 20) p -= 8; dma16((const char*)(wq + (size_t)i40 * 16 * D), p * 1024 + lane * 16, KV_RED + p * 1024); }
  unsigned vo[5];
#pragma unroll
  for (int i = 0; i < 5; ++i) {
    unsigned p = wid + 8 * i; if (p >= 35) p -= 8;
    if (p < 21) { const unsigned P = p * 64 + lane, row = P / 21, c = P - row * 21; vo[i] = row * (D * 2) + (c < 20 ? c : 19) * 16; }
    else { unsigned of = ((p - 21) % 7) * 1024 + lane * 16; vo[i] = of < 6400 ? of : 0; }
  }
  auto item_of = [&](int n) { return n < 3 ? i40 + 40 * n : (kv ? 1000 : 120 + (i40 - 32) + 8 * (n - 3)); };
  if (kv) {
#pragma unroll
    for (int quarter = 0; quarter < 4; ++quarter)
#pragma unroll
      for (int i = 0; i < 5; ++i) {
        unsigned p = wid + 8 * i; if (p >= 35) p -= 8;
        const char* src = p < 21 ? xr + quarter * 320 : (p < 28 ? (const char*)wk : (const char*)wv) + quarter * 6400;
        const unsigned dst = p < 21 ? quarter * QS_W + p * 1024 : (p < 28 ? KV_WK + (p - 21) * 1024 : KV_WV + (p - 28) * 1024) + quarter * 7168;
        if (p != 27 || lane < 16) dma16(src, vo[i], dst);
      }
    if (wid < 4 && lane < 20) dma16((const char*)gate_w + wid * 320, lane * 16, KV_WK + wid * 7168 + KV_GWO);
  } else {
#pragma unroll
    for (int n = 0; n < 7; ++n) if (item_of(n) < 148) pack_issue(wd, wo, item_of(n), n * 20480, lane, wid);
  }
  const unsigned tid = wid * 64 + lane;
  if (kv) WAITV(20); else WAITV(18);
  BARRIER();
  packq_store(wsr, i40, KV_RED, lane, wid);
  WAITV(0); BARRIER();
  asm volatile("" : "+v"(ep));
  const unsigned tag = rfl(ep) + 1u;
  if (tid == 4) flag_set(ws, WS_FWQ + i40 * 128, tag);
  if (kv) {
    const unsigned isv = wid >> 2, kg = wid & 3;
    f32x16 ac2[2] = {};
    {
      const unsigned ax = kg * QS_W + l31 * 336 + hh * 16;
      const unsigned aw = (isv ? KV_WV : KV_WK) + kg * 7168 + (8 * hh + q4) * 40 + 32 * b16 + 8 * p4;
      const unsigned ag = KV_WK + kg * 7168 + KV_GWO + 16 * hh;
#pragma unroll
      for (int half = 0; half < 2; ++half) {
        bf16x8 w[5], xf[5][2];
#pragma unroll
        for (int k = 0; k < 5; ++k) {
          const int ks = half * 5 + k;
          w[k] = cat4(lds_tr(aw + ks * 640), lds_tr(aw + ks * 640 + 160));
          if (!isv) { const bf16x8 gw = lds_b128(ag + ks * 32); if (l31 == 20) w[k] = gw; }
          xf[k][0] = lds_b128(ax + ks * 32); xf[k][1] = lds_b128(ax + 32 * 336 + ks * 32);
        }
        if (!isv) {
#pragma unroll
          for (int k = 0; k < 5; ++k) { ac2[0] = mfma32(w[k], xf[k][0], ac2[0]); ac2[1] = mfma32(w[k], xf[k][1], ac2[1]); }
        } else {
#pragma unroll
          for (int k = 0; k < 5; ++k) { ac2[0] = mfma32(xf[k][0], w[k], ac2[0]); ac2[1] = mfma32(xf[k][1], w[k], ac2[1]); }
        }
      }
    }
    BARRIER();
    constexpr unsigned RED = KV_WK;
#pragma unroll
    for (int t = 0; t < 2; ++t)
#pragma unroll
      for (int g = 0; g < 4; ++g) *(LDSP f32x4*)(size_t)(RED + (((isv * 2 + t) * 4 + kg) * 4 + g) * 1024 + lane * 16) = f32x4{ac2[t][4 * g], ac2[t][4 * g + 1], ac2[t][4 * g + 2], ac2[t][4 * g + 3]};
    BARRIER();
    if (kg < 2) {
      const unsigned mt = kg;
      f32x16 acc = {};
#pragma unroll
      for (int j = 0; j < 4; ++j)
#pragma unroll
        for (int g = 0; g < 4; ++g) { const f32x4 v = *(LDSP const f32x4*)(size_t)(RED + (((isv * 2 + mt) * 4 + j) * 4 + g) * 1024 + lane * 16); acc[4 * g] += v[0]; acc[4 * g + 1] += v[1]; acc[4 * g + 2] += v[2]; acc[4 * g + 3] += v[3]; }
      const int key0 = i40 * 64 + mt * 32;
      const unsigned kvo = WS_KV + (key0 >> 8) * KV_CHUNK;
      const int t32 = (key0 >> 5) & 7;
      if (!isv) {
        unsigned pk[6];
#pragma unroll
        for (int r = 0; r < 12; r += 2) pk[r >> 1] = pk2(acc[r], acc[r + 1]);
        bst128(u32x4{pk[0], pk[1], pk[2], pk[3]}, wsr, lane * 16, kvo + t32 * 1024);
        bst64(hh == 0 ? u32x2{pk[4], pk[5]} : u32x2{0x3f803f80u, 0x00003f80u}, wsr, lane * 8, kvo + KV_KB + t32 * 512);
        if (hh == 1) bst32(__float_as_uint(1.0f / (1.0f + __expf(-acc[8]))), wsr, l31 * 4, WS_G + key0 * 4);
        float n2 = 0.f;
#pragma unroll
        for (int i = 0; i < 6; ++i) {
          if (i < 4 || hh == 0) { float a = __uint_as_float(pk[i] << 16), b = __uint_as_float(pk[i] & 0xffff0000u); n2 += a * a + b * b; }
        }
        n2 = wave_max_pos(half_sum(n2));
        if (lane == 0) bst32(__float_as_uint(n2), wsr, 0, WS_KM + (i40 * 2 + mt) * 4);
      } else if (l31 <= 20) {
#pragma unroll
        for (int g = 0; g < 4; ++g) {
          u32x2 v = {pk2(acc[4 * g], acc[4 * g + 1]), pk2(acc[4 * g + 2], acc[4 * g + 3])};
          if (l31 == 20) v = u32x2{0x3f803f80u, 0x3f803f80u};
          bst64(v, wsr, l31 * 16 + hh * 8, kvo + KV_V + (t32 * 2 + (g >> 1)) * 672 + (g & 1) * 336);
        }
      }
    }
    BARRIER();
#pragma unroll
    for (int n = 0; n < 3; ++n) pack_issue(wd, wo, item_of(n), KV_WK + n * 20480, lane, wid);
    WAITV(9); BARRIER();
    if (tid < 4) flag_set(ws, WS_FKV + (tid * 32 + i40) * 128, tag);
    if (wid == 1) poll_flags(ws, WS_FWQ + (lane < 40 ? lane : 39) * 128, true, tag);
    BARRIER();
    const unsigned rg = wid & 3, qp = wid >> 2;
    bf16x8 wf[20];
#pragma unroll
    for (int m = 0; m < 20; ++m) wf[m] = bld128(wsr, (hh * 20 + (l31 < 20 ? l31 : 19)) * 16, WS_WQ + ((hg * 80 + 2 * m + qp) * 4 + rg) * 640);
    SB();
    f32x16 qacc[2] = {};
#pragma unroll
    for (int qn = 0; qn < 4; ++qn) {
      bf16x8 xf[5][2];
#pragma unroll
      for (int k = 0; k < 5; ++k) { const unsigned ax = qn * QS_W + l31 * 336 + hh * 16 + (2 * k + qp) * 32; xf[k][0] = lds_b128(ax); xf[k][1] = lds_b128(ax + 32 * 336); }
      SB();
#pragma unroll
      for (int k = 0; k < 5; ++k) { qacc[0] = mfma32(wf[qn * 5 + k], xf[k][0], qacc[0]); qacc[1] = mfma32(wf[qn * 5 + k], xf[k][1], qacc[1]); }
    }
    q_finish<true>(bp, qacc, 0, lane, wid);
    if (wid == 0) poll_flags(ws, WS_FKV + (rep * 32 + (lane & 31)) * 128, lane < 32, tag);
#pragma unroll
    for (int n = 0; n < 3; ++n) pack_store(wsr, item_of(n), KV_WK + n * 20480, lane, wid);
    BARRIER();
    mark_of(wsr, qb, hg, lane, wid);
#pragma unroll
    for (int i = 0; i < 6; ++i) kv_piece(wsr, lane, wid, i);
    kv_scalars(bp, wsr, qb, lane, wid);
    WAITV(9); BARRIER();
    if (wid == 0 && lane == 7) flag_set(ws, WS_FARR + blockIdx.x * 128, tag);
  } else {
#pragma unroll
    for (int n = 0; n < 7; ++n) if (item_of(n) < 148) pack_store(wsr, item_of(n), n * 20480, lane, wid);
    WAITV(0); BARRIER();
  }
  if (wid == 1 && lane < 7 && item_of(lane) < 148) flag_set(ws, WS_FW + item_of(lane) * 128, tag);
  return tag;
}

#define ATT_LOOP_SC1 \
  "v_mov_b32_e32 v244, 0xdddddddd\n\t" \
  "v_mov_b32_e32 v96, 0\n\t" \
  "v_mov_b32_e32 v97, 0\n\t" \
  "v_mov_b32_e32 v98, 0\n\t" \
  "v_mov_b32_e32 v99, 0\n\t" \
  "v_mov_b32_e32 v100, 0\n\t" \
  "v_mov_b32_e32 v101, 0\n\t" \
  "v_mov_b32_e32 v102, 0\n\t" \
  "v_mov_b32_e32 v103, 0\n\t" \
  "v_mov_b32_e32 v104, 0\n\t" \
  "v_mov_b32_e32 v105, 0\n\t" \
  "v_mov_b32_e32 v106, 0\n\t" \
  "v_mov_b32_e32 v107, 0\n\t" \
  "v_mov_b32_e32 v108, 0\n\t" \
  "v_mov_b32_e32 v109, 0\n\t" \
  "v_mov_b32_e32 v110, 0\n\t" \
  "v_mov_b32_e32 v111, 0\n\t" \
  "v_mov_b32_e32 v112, 0\n\t" \
  "v_mov_b32_e32 v113, 0\n\t" \
  "v_mov_b32_e32 v114, 0\n\t" \
  "v_mov_b32_e32 v115, 0\n\t" \
  "v_mov_b32_e32 v116, 0\n\t" \
  "v_mov_b32_e32 v117, 0\n\t" \
  "v_mov_b32_e32 v118, 0\n\t" \
  "v_mov_b32_e32 v119, 0\n\t" \
  "v_mov_b32_e32 v120, 0\n\t" \
  "v_mov_b32_e32 v121, 0\n\t" \
  "v_mov_b32_e32 v122, 0\n\t" \
  "v_mov_b32_e32 v123, 0\n\t" \
  "v_mov_b32_e32 v124, 0\n\t" \
  "v_mov_b32_e32 v125, 0\n\t" \
  "v_mov_b32_e32 v126, 0\n\t" \
  "v_mov_b32_e32 v127, 0\n\t" \
  "ds_read_b128 v[188:191], v246 offset:0\n\t" \
  "ds_read_b64 v[196:197], v247 offset:0\n\t" \
  "ds_read_b128 v[192:195], v246 offset:1024\n\t" \
  "ds_read_b64 v[198:199], v247 offset:512\n\t" \
  "ds_read_b128 v[200:203], v248 offset:0\n\t" \
  "ds_read_b128 v[208:211], v248 offset:672\n\t" \
  "s_waitcnt lgkmcnt(4)\n\t" \
  "v_mfma_f32_32x32x16_bf16 v[32:47], v[188:191], v[176:179], 0\n\t" \
  "v_mfma_f32_32x32x16_bf16 v[48:63], v[188:191], v[180:183], 0\n\t" \
  "v_mfma_f32_32x32x8_bf16 v[32:47], v[196:197], v[184:185], v[32:47]\n\t" \
  "v_mfma_f32_32x32x8_bf16 v[48:63], v[196:197], v[186:187], v[48:63]\n\t" \
  "s_nop 7\n\t" \
  "s_nop 7\n\t" \
  "s_waitcnt lgkmcnt(0)\n\t" \
  "v_mfma_f32_32x32x16_bf16 v[64:79], v[192:195], v[176:179], 0\n\t" \
  "v_exp_f32_e32 v32, v32\n\t" \
  "v_exp_f32_e32 v33, v33\n\t" \
  "v_exp_f32_e32 v34, v34\n\t" \
  "v_exp_f32_e32 v35, v35\n\t" \
  "ds_read_b128 v[188:191], v246 offset:2048\n\t" \
  "ds_read_b64 v[196:197], v247 offset:1024\n\t" \
  "s_add_u32 m0, %[dlds], 0\n\t" \
  "s_sub_u32 %[t0], %[dsrc], 49152\n\t" \
  "buffer_load_dwordx4 v245, %[srd], %[t0] offen sc1 lds\n\t" \
  "s_add_u32 m0, %[dlds], 4096\n\t" \
  "s_sub_u32 %[t0], %[dsrc], 45056\n\t" \
  "buffer_load_dwordx4 v245, %[srd], %[t0] offen sc1 lds\n\t" \
  "v_exp_f32_e32 v36, v36\n\t" \
  "v_exp_f32_e32 v37, v37\n\t" \
  "v_exp_f32_e32 v38, v38\n\t" \
  "v_exp_f32_e32 v39, v39\n\t" \
  "ds_read_b128 v[212:215], v248 offset:2016\n\t" \
  "v_mfma_f32_32x32x8_bf16 v[64:79], v[198:199], v[184:185], v[64:79]\n\t" \
  "v_exp_f32_e32 v40, v40\n\t" \
  "v_exp_f32_e32 v41, v41\n\t" \
  "v_exp_f32_e32 v42, v42\n\t" \
  "v_exp_f32_e32 v43, v43\n\t" \
  "ds_read_b128 v[204:207], v248 offset:1344\n\t" \
  "v_smfmac_f32_32x32x32_bf16 v[96:111], v[200:203], v[32:39], v244\n\t" \
  "v_exp_f32_e32 v44, v44\n\t" \
  "v_exp_f32_e32 v45, v45\n\t" \
  "v_exp_f32_e32 v46, v46\n\t" \
  "v_exp_f32_e32 v47, v47\n\t" \
  "v_mfma_f32_32x32x16_bf16 v[80:95], v[192:195], v[180:183], 0\n\t" \
  "v_exp_f32_e32 v48, v48\n\t" \
  "v_exp_f32_e32 v49, v49\n\t" \
  "v_exp_f32_e32 v50, v50\n\t" \
  "v_exp_f32_e32 v51, v51\n\t" \
  "v_smfmac_f32_32x32x32_bf16 v[96:111], v[208:211], v[40:47], v244\n\t" \
  "v_exp_f32_e32 v52, v52\n\t" \
  "v_exp_f32_e32 v53, v53\n\t" \
  "v_exp_f32_e32 v54, v54\n\t" \
  "v_exp_f32_e32 v55, v55\n\t" \
  "v_mfma_f32_32x32x8_bf16 v[80:95], v[198:199], v[186:187], v[80:95]\n\t" \
  "v_exp_f32_e32 v56, v56\n\t" \
  "v_exp_f32_e32 v57, v57\n\t" \
  "v_exp_f32_e32 v58, v58\n\t" \
  "v_exp_f32_e32 v59, v59\n\t" \
  "v_smfmac_f32_32x32x32_bf16 v[112:127], v[200:203], v[48:55], v244\n\t" \
  "v_exp_f32_e32 v60, v60\n\t" \
  "v_exp_f32_e32 v61, v61\n\t" \
  "v_exp_f32_e32 v62, v62\n\t" \
  "v_exp_f32_e32 v63, v63\n\t" \
  "s_waitcnt lgkmcnt(0)\n\t" \
  "v_mfma_f32_32x32x16_bf16 v[32:47], v[188:191], v[176:179], 0\n\t" \
  "v_exp_f32_e32 v64, v64\n\t" \
  "v_exp_f32_e32 v65, v65\n\t" \
  "v_exp_f32_e32 v66, v66\n\t" \
  "v_exp_f32_e32 v67, v67\n\t" \
  "ds_read_b128 v[192:195], v246 offset:3072\n\t" \
  "ds_read_b64 v[198:199], v247 offset:1536\n\t" \
  "s_add_u32 m0, %[dlds], 8192\n\t" \
  "s_sub_u32 %[t0], %[dsrc], 40960\n\t" \
  "buffer_load_dwordx4 v245, %[srd], %[t0] offen sc1 lds\n\t" \
  "s_add_u32 m0, %[dlds], 12288\n\t" \
  "s_sub_u32 %[t0], %[dsrc], 36864\n\t" \
  "buffer_load_dwordx4 v245, %[srd], %[t0] offen sc1 lds\n\t" \
  "v_smfmac_f32_32x32x32_bf16 v[112:127], v[208:211], v[56:63], v244\n\t" \
  "v_exp_f32_e32 v68, v68\n\t" \
  "v_exp_f32_e32 v69, v69\n\t" \
  "v_exp_f32_e32 v70, v70\n\t" \
  "v_exp_f32_e32 v71, v71\n\t" \
  "ds_read_b128 v[208:211], v248 offset:3360\n\t" \
  "v_mfma_f32_32x32x8_bf16 v[32:47], v[196:197], v[184:185], v[32:47]\n\t" \
  "v_exp_f32_e32 v72, v72\n\t" \
  "v_exp_f32_e32 v73, v73\n\t" \
  "v_exp_f32_e32 v74, v74\n\t" \
  "v_exp_f32_e32 v75, v75\n\t" \
  "ds_read_b128 v[200:203], v248 offset:2688\n\t" \
  "v_smfmac_f32_32x32x32_bf16 v[96:111], v[204:207], v[64:71], v244\n\t" \
  "v_exp_f32_e32 v76, v76\n\t" \
  "v_exp_f32_e32 v77, v77\n\t" \
  "v_exp_f32_e32 v78, v78\n\t" \
  "v_exp_f32_e32 v79, v79\n\t" \
  "v_mfma_f32_32x32x16_bf16 v[48:63], v[188:191], v[180:183], 0\n\t" \
  "v_exp_f32_e32 v80, v80\n\t" \
  "v_exp_f32_e32 v81, v81\n\t" \
  "v_exp_f32_e32 v82, v82\n\t" \
  "v_exp_f32_e32 v83, v83\n\t" \
  "v_smfmac_f32_32x32x32_bf16 v[96:111], v[212:215], v[72:79], v244\n\t" \
  "v_exp_f32_e32 v84, v84\n\t" \
  "v_exp_f32_e32 v85, v85\n\t" \
  "v_exp_f32_e32 v86, v86\n\t" \
  "v_exp_f32_e32 v87, v87\n\t" \
  "v_mfma_f32_32x32x8_bf16 v[48:63], v[196:197], v[186:187], v[48:63]\n\t" \
  "v_exp_f32_e32 v88, v88\n\t" \
  "v_exp_f32_e32 v89, v89\n\t" \
  "v_exp_f32_e32 v90, v90\n\t" \
  "v_exp_f32_e32 v91, v91\n\t" \
  "v_smfmac_f32_32x32x32_bf16 v[112:127], v[204:207], v[80:87], v244\n\t" \
  "v_exp_f32_e32 v92, v92\n\t" \
  "v_exp_f32_e32 v93, v93\n\t" \
  "v_exp_f32_e32 v94, v94\n\t" \
  "v_exp_f32_e32 v95, v95\n\t" \
  "s_waitcnt lgkmcnt(0)\n\t" \
  "buffer_load_dword v216, v232, %[srd], 0 offen sc1\n\t" \
  "v_mfma_f32_32x32x16_bf16 v[64:79], v[192:195], v[176:179], 0\n\t" \
  "v_exp_f32_e32 v32, v32\n\t" \
  "v_exp_f32_e32 v33, v33\n\t" \
  "v_exp_f32_e32 v34, v34\n\t" \
  "v_exp_f32_e32 v35, v35\n\t" \
  "ds_read_b128 v[188:191], v246 offset:4096\n\t" \
  "ds_read_b64 v[196:197], v247 offset:2048\n\t" \
  "s_add_u32 m0, %[dlds], 16384\n\t" \
  "s_sub_u32 %[t0], %[dsrc], 32768\n\t" \
  "buffer_load_dwordx4 v245, %[srd], %[t0] offen sc1 lds\n\t" \
  "s_add_u32 m0, %[dlds], 20480\n\t" \
  "s_sub_u32 %[t0], %[dsrc], 28672\n\t" \
  "buffer_load_dwordx4 v245, %[srd], %[t0] offen sc1 lds\n\t" \
  "v_smfmac_f32_32x32x32_bf16 v[112:127], v[212:215], v[88:95], v244\n\t" \
  "v_exp_f32_e32 v36, v36\n\t" \
  "v_exp_f32_e32 v37, v37\n\t" \
  "v_exp_f32_e32 v38, v38\n\t" \
  "v_exp_f32_e32 v39, v39\n\t" \
  "ds_read_b128 v[212:215], v248 offset:4704\n\t" \
  "v_mfma_f32_32x32x8_bf16 v[64:79], v[198:199], v[184:185], v[64:79]\n\t" \
  "v_exp_f32_e32 v40, v40\n\t" \
  "v_exp_f32_e32 v41, v41\n\t" \
  "v_exp_f32_e32 v42, v42\n\t" \
  "v_exp_f32_e32 v43, v43\n\t" \
  "ds_read_b128 v[204:207], v248 offset:4032\n\t" \
  "v_smfmac_f32_32x32x32_bf16 v[96:111], v[200:203], v[32:39], v244\n\t" \
  "v_exp_f32_e32 v44, v44\n\t" \
  "v_exp_f32_e32 v45, v45\n\t" \
  "v_exp_f32_e32 v46, v46\n\t" \
  "v_exp_f32_e32 v47, v47\n\t" \
  "v_mfma_f32_32x32x16_bf16 v[80:95], v[192:195], v[180:183], 0\n\t" \
  "v_exp_f32_e32 v48, v48\n\t" \
  "v_exp_f32_e32 v49, v49\n\t" \
  "v_exp_f32_e32 v50, v50\n\t" \
  "v_exp_f32_e32 v51, v51\n\t" \
  "v_smfmac_f32_32x32x32_bf16 v[96:111], v[208:211], v[40:47], v244\n\t" \
  "v_exp_f32_e32 v52, v52\n\t" \
  "v_exp_f32_e32 v53, v53\n\t" \
  "v_exp_f32_e32 v54, v54\n\t" \
  "v_exp_f32_e32 v55, v55\n\t" \
  "v_mfma_f32_32x32x8_bf16 v[80:95], v[198:199], v[186:187], v[80:95]\n\t" \
  "v_exp_f32_e32 v56, v56\n\t" \
  "v_exp_f32_e32 v57, v57\n\t" \
  "v_exp_f32_e32 v58, v58\n\t" \
  "v_exp_f32_e32 v59, v59\n\t" \
  "v_smfmac_f32_32x32x32_bf16 v[112:127], v[200:203], v[48:55], v244\n\t" \
  "v_exp_f32_e32 v60, v60\n\t" \
  "v_exp_f32_e32 v61, v61\n\t" \
  "v_exp_f32_e32 v62, v62\n\t" \
  "v_exp_f32_e32 v63, v63\n\t" \
  "s_waitcnt lgkmcnt(0)\n\t" \
  "v_mfma_f32_32x32x16_bf16 v[32:47], v[188:191], v[176:179], 0\n\t" \
  "v_exp_f32_e32 v64, v64\n\t" \
  "v_exp_f32_e32 v65, v65\n\t" \
  "v_exp_f32_e32 v66, v66\n\t" \
  "v_exp_f32_e32 v67, v67\n\t" \
  "ds_read_b128 v[192:195], v246 offset:5120\n\t" \
  "ds_read_b64 v[198:199], v247 offset:2560\n\t" \
  "s_add_u32 m0, %[dlds], 49152\n\t" \
  "s_sub_u32 %[t0], %[dsrc], 24576\n\t" \
  "buffer_load_dwordx4 v245, %[srd], %[t0] offen sc1 lds\n\t" \
  "v_smfmac_f32_32x32x32_bf16 v[112:127], v[208:211], v[56:63], v244\n\t" \
  "v_exp_f32_e32 v68, v68\n\t" \
  "v_exp_f32_e32 v69, v69\n\t" \
  "v_exp_f32_e32 v70, v70\n\t" \
  "v_exp_f32_e32 v71, v71\n\t" \
  "ds_read_b128 v[208:211], v248 offset:6048\n\t" \
  "v_mfma_f32_32x32x8_bf16 v[32:47], v[196:197], v[184:185], v[32:47]\n\t" \
  "v_exp_f32_e32 v72, v72\n\t" \
  "v_exp_f32_e32 v73, v73\n\t" \
  "v_exp_f32_e32 v74, v74\n\t" \
  "v_exp_f32_e32 v75, v75\n\t" \
  "ds_read_b128 v[200:203], v248 offset:5376\n\t" \
  "v_smfmac_f32_32x32x32_bf16 v[96:111], v[204:207], v[64:71], v244\n\t" \
  "v_exp_f32_e32 v76, v76\n\t" \
  "v_exp_f32_e32 v77, v77\n\t" \
  "v_exp_f32_e32 v78, v78\n\t" \
  "v_exp_f32_e32 v79, v79\n\t" \
  "v_mfma_f32_32x32x16_bf16 v[48:63], v[188:191], v[180:183], 0\n\t" \
  "v_exp_f32_e32 v80, v80\n\t" \
  "v_exp_f32_e32 v81, v81\n\t" \
  "v_exp_f32_e32 v82, v82\n\t" \
  "v_exp_f32_e32 v83, v83\n\t" \
  "v_smfmac_f32_32x32x32_bf16 v[96:111], v[212:215], v[72:79], v244\n\t" \
  "v_exp_f32_e32 v84, v84\n\t" \
  "v_exp_f32_e32 v85, v85\n\t" \
  "v_exp_f32_e32 v86, v86\n\t" \
  "v_exp_f32_e32 v87, v87\n\t" \
  "v_mfma_f32_32x32x8_bf16 v[48:63], v[196:197], v[186:187], v[48:63]\n\t" \
  "v_exp_f32_e32 v88, v88\n\t" \
  "v_exp_f32_e32 v89, v89\n\t" \
  "v_exp_f32_e32 v90, v90\n\t" \
  "v_exp_f32_e32 v91, v91\n\t" \
  "v_smfmac_f32_32x32x32_bf16 v[112:127], v[204:207], v[80:87], v244\n\t" \
  "v_exp_f32_e32 v92, v92\n\t" \
  "v_exp_f32_e32 v93, v93\n\t" \
  "v_exp_f32_e32 v94, v94\n\t" \
  "v_exp_f32_e32 v95, v95\n\t" \
  "s_waitcnt lgkmcnt(0)\n\t" \
  "s_waitcnt vmcnt(3)\n\t" \
  "s_mov_b32 %[t0], 0\n\t" \
  "L_poll_%=:\n\t" \
  "v_cmp_ne_u32_e32 vcc, %[tag], v216\n\t" \
  "s_cmp_lt_u32 %[t0], 0x100000\n\t" \
  "s_cselect_b64 %[tp], vcc, 0\n\t" \
  "s_cmp_lg_u64 %[tp], 0\n\t" \
  "s_cbranch_scc0 L_polled_%=\n\t" \
  "buffer_load_dword v216, v232, %[srd], 0 offen sc1\n\t" \
  "s_add_u32 %[t0], %[t0], 1\n\t" \
  "s_waitcnt vmcnt(0)\n\t" \
  "s_branch L_poll_%=\n\t" \
  "L_polled_%=:\n\t" \
  "s_barrier\n\t" \
  "buffer_load_dwordx4 v[220:223], v245, %[srd], %[fxo] offen sc1\n\t" \
  "s_add_u32 %[fxo], %[fxo], 0x400\n\t" \
  "buffer_load_dwordx4 v[224:227], v245, %[srd], %[fxo] offen sc1\n\t" \
  "s_add_u32 %[fxo], %[fxo], 0x400\n\t" \
  "buffer_load_dwordx4 v[228:231], v245, %[srd], %[fxo] offen sc1\n\t" \
  "s_add_u32 %[fxo], %[fxo], 0x400\n\t" \
  "v_mfma_f32_32x32x16_bf16 v[64:79], v[192:195], v[176:179], 0\n\t" \
  "v_exp_f32_e32 v32, v32\n\t" \
  "v_exp_f32_e32 v33, v33\n\t" \
  "v_exp_f32_e32 v34, v34\n\t" \
  "v_exp_f32_e32 v35, v35\n\t" \
  "ds_read_b128 v[188:191], v246 offset:6144\n\t" \
  "ds_read_b64 v[196:197], v247 offset:3072\n\t" \
  "v_smfmac_f32_32x32x32_bf16 v[112:127], v[212:215], v[88:95], v244\n\t" \
  "v_exp_f32_e32 v36, v36\n\t" \
  "v_exp_f32_e32 v37, v37\n\t" \
  "v_exp_f32_e32 v38, v38\n\t" \
  "v_exp_f32_e32 v39, v39\n\t" \
  "ds_read_b128 v[212:215], v248 offset:7392\n\t" \
  "v_mfma_f32_32x32x8_bf16 v[64:79], v[198:199], v[184:185], v[64:79]\n\t" \
  "v_exp_f32_e32 v40, v40\n\t" \
  "v_exp_f32_e32 v41, v41\n\t" \
  "v_exp_f32_e32 v42, v42\n\t" \
  "v_exp_f32_e32 v43, v43\n\t" \
  "ds_read_b128 v[204:207], v248 offset:6720\n\t" \
  "v_smfmac_f32_32x32x32_bf16 v[96:111], v[200:203], v[32:39], v244\n\t" \
  "v_exp_f32_e32 v44, v44\n\t" \
  "v_exp_f32_e32 v45, v45\n\t" \
  "v_exp_f32_e32 v46, v46\n\t" \
  "v_exp_f32_e32 v47, v47\n\t" \
  "v_mfma_f32_32x32x16_bf16 v[80:95], v[192:195], v[180:183], 0\n\t" \
  "v_exp_f32_e32 v48, v48\n\t" \
  "v_exp_f32_e32 v49, v49\n\t" \
  "v_exp_f32_e32 v50, v50\n\t" \
  "v_exp_f32_e32 v51, v51\n\t" \
  "v_smfmac_f32_32x32x32_bf16 v[96:111], v[208:211], v[40:47], v244\n\t" \
  "v_exp_f32_e32 v52, v52\n\t" \
  "v_exp_f32_e32 v53, v53\n\t" \
  "v_exp_f32_e32 v54, v54\n\t" \
  "v_exp_f32_e32 v55, v55\n\t" \
  "v_mfma_f32_32x32x8_bf16 v[80:95], v[198:199], v[186:187], v[80:95]\n\t" \
  "v_exp_f32_e32 v56, v56\n\t" \
  "v_exp_f32_e32 v57, v57\n\t" \
  "v_exp_f32_e32 v58, v58\n\t" \
  "v_exp_f32_e32 v59, v59\n\t" \
  "buffer_load_dwordx4 v[232:235], v245, %[srd], %[fwo] offen sc1\n\t" \
  "buffer_load_dwordx4 v[236:239], v245, %[srd], %[fwo] offen offset:1024 sc1\n\t" \
  "buffer_load_dwordx4 v[240:243], v252, %[srd], %[fwo] offen offset:2048 sc1\n\t" \
  "s_add_u32 %[fwo], %[fwo], 0xc00\n\t" \
  "v_smfmac_f32_32x32x32_bf16 v[112:127], v[200:203], v[48:55], v244\n\t" \
  "v_exp_f32_e32 v60, v60\n\t" \
  "v_exp_f32_e32 v61, v61\n\t" \
  "v_exp_f32_e32 v62, v62\n\t" \
  "v_exp_f32_e32 v63, v63\n\t" \
  "s_waitcnt lgkmcnt(0)\n\t" \
  "v_mfma_f32_32x32x16_bf16 v[32:47], v[188:191], v[176:179], 0\n\t" \
  "v_exp_f32_e32 v64, v64\n\t" \
  "v_exp_f32_e32 v65, v65\n\t" \
  "v_exp_f32_e32 v66, v66\n\t" \
  "ds_read_b128 v[192:195], v246 offset:7168\n\t" \
  "ds_read_b64 v[198:199], v247 offset:3584\n\t" \
  "s_add_u32 m0, %[dlds], 53248\n\t" \
  "s_sub_u32 %[t0], %[dsrc], 20480\n\t" \
  "buffer_load_dwordx4 v245, %[srd], %[t0] offen sc1 lds\n\t" \
  "buffer_load_dwordx4 v[216:219], v245, %[srd], %[fxo] offen sc1\n\t" \
  "s_add_u32 %[fxo], %[fxo], 0x400\n\t" \
  "v_smfmac_f32_32x32x32_bf16 v[112:127], v[208:211], v[56:63], v244\n\t" \
  "v_exp_f32_e32 v67, v67\n\t" \
  "v_exp_f32_e32 v68, v68\n\t" \
  "ds_read_b128 v[208:211], v248 offset:8736\n\t" \
  "v_mfma_f32_32x32x8_bf16 v[32:47], v[196:197], v[184:185], v[32:47]\n\t" \
  "v_exp_f32_e32 v69, v69\n\t" \
  "v_exp_f32_e32 v70, v70\n\t" \
  "v_exp_f32_e32 v71, v71\n\t" \
  "v_exp_f32_e32 v72, v72\n\t" \
  "s_waitcnt vmcnt(4)\n\t" \
  "v_mfma_f32_32x32x16_bf16 v[128:143], v[232:235], v[220:223], 0\n\t" \
  "v_exp_f32_e32 v73, v73\n\t" \
  "v_exp_f32_e32 v74, v74\n\t" \
  "v_exp_f32_e32 v75, v75\n\t" \
  "buffer_load_dwordx4 v[232:235], v245, %[srd], %[fwo] offen sc1\n\t" \
  "v_smfmac_f32_32x32x32_bf16 v[96:111], v[204:207], v[64:71], v244\n\t" \
  "v_exp_f32_e32 v76, v76\n\t" \
  "v_exp_f32_e32 v77, v77\n\t" \
  "ds_read_b128 v[200:203], v248 offset:8064\n\t" \
  "v_mfma_f32_32x32x16_bf16 v[48:63], v[188:191], v[180:183], 0\n\t" \
  "v_exp_f32_e32 v78, v78\n\t" \
  "v_exp_f32_e32 v79, v79\n\t" \
  "v_exp_f32_e32 v80, v80\n\t" \
  "s_waitcnt vmcnt(4)\n\t" \
  "v_mfma_f32_32x32x16_bf16 v[144:159], v[236:239], v[220:223], 0\n\t" \
  "v_exp_f32_e32 v81, v81\n\t" \
  "v_exp_f32_e32 v82, v82\n\t" \
  "v_exp_f32_e32 v83, v83\n\t" \
  "buffer_load_dwordx4 v[236:239], v245, %[srd], %[fwo] offen offset:1024 sc1\n\t" \
  "v_smfmac_f32_32x32x32_bf16 v[96:111], v[212:215], v[72:79], v244\n\t" \
  "v_exp_f32_e32 v84, v84\n\t" \
  "v_exp_f32_e32 v85, v85\n\t" \
  "v_mfma_f32_32x32x8_bf16 v[48:63], v[196:197], v[186:187], v[48:63]\n\t" \
  "v_exp_f32_e32 v86, v86\n\t" \
  "v_exp_f32_e32 v87, v87\n\t" \
  "v_exp_f32_e32 v88, v88\n\t" \
  "v_exp_f32_e32 v89, v89\n\t" \
  "s_waitcnt vmcnt(4)\n\t" \
  "v_mfma_f32_32x32x16_bf16 v[160:175], v[240:243], v[220:223], 0\n\t" \
  "v_exp_f32_e32 v90, v90\n\t" \
  "v_exp_f32_e32 v91, v91\n\t" \
  "v_exp_f32_e32 v92, v92\n\t" \
  "buffer_load_dwordx4 v[240:243], v252, %[srd], %[fwo] offen offset:2048 sc1\n\t" \
  "s_add_u32 %[fwo], %[fwo], 0xc00\n\t" \
  "v_smfmac_f32_32x32x32_bf16 v[112:127], v[204:207], v[80:87], v244\n\t" \
  "v_exp_f32_e32 v93, v93\n\t" \
  "v_exp_f32_e32 v94, v94\n\t" \
  "v_exp_f32_e32 v95, v95\n\t" \
  "s_waitcnt lgkmcnt(0)\n\t" \
  "s_barrier\n\t" \
  "v_mfma_f32_32x32x16_bf16 v[64:79], v[192:195], v[176:179], 0\n\t" \
  "v_exp_f32_e32 v32, v32\n\t" \
  "v_exp_f32_e32 v33, v33\n\t" \
  "v_exp_f32_e32 v34, v34\n\t" \
  "ds_read_b128 v[188:191], v249 offset:0\n\t" \
  "ds_read_b64 v[196:197], v250 offset:0\n\t" \
  "s_add_u32 m0, %[dlds], 57344\n\t" \
  "s_sub_u32 %[t0], %[dsrc], 16384\n\t" \
  "buffer_load_dwordx4 v245, %[srd], %[t0] offen sc1 lds\n\t" \
  "buffer_load_dwordx4 v[220:223], v245, %[srd], %[fxo] offen sc1\n\t" \
  "s_add_u32 %[fxo], %[fxo], 0x400\n\t" \
  "v_smfmac_f32_32x32x32_bf16 v[112:127], v[212:215], v[88:95], v244\n\t" \
  "v_exp_f32_e32 v35, v35\n\t" \
  "v_exp_f32_e32 v36, v36\n\t" \
  "ds_read_b128 v[212:215], v248 offset:10080\n\t" \
  "v_mfma_f32_32x32x8_bf16 v[64:79], v[198:199], v[184:185], v[64:79]\n\t" \
  "v_exp_f32_e32 v37, v37\n\t" \
  "v_exp_f32_e32 v38, v38\n\t" \
  "v_exp_f32_e32 v39, v39\n\t" \
  "v_exp_f32_e32 v40, v40\n\t" \
  "s_waitcnt vmcnt(4)\n\t" \
  "v_mfma_f32_32x32x16_bf16 v[128:143], v[232:235], v[224:227], v[128:143]\n\t" \
  "v_exp_f32_e32 v41, v41\n\t" \
  "v_exp_f32_e32 v42, v42\n\t" \
  "v_exp_f32_e32 v43, v43\n\t" \
  "buffer_load_dwordx4 v[232:235], v245, %[srd], %[fwo] offen sc1\n\t" \
  "v_smfmac_f32_32x32x32_bf16 v[96:111], v[200:203], v[32:39], v244\n\t" \
  "v_exp_f32_e32 v44, v44\n\t" \
  "v_exp_f32_e32 v45, v45\n\t" \
  "ds_read_b128 v[204:207], v248 offset:9408\n\t" \
  "v_mfma_f32_32x32x16_bf16 v[80:95], v[192:195], v[180:183], 0\n\t" \
  "v_exp_f32_e32 v46, v46\n\t" \
  "v_exp_f32_e32 v47, v47\n\t" \
  "v_exp_f32_e32 v48, v48\n\t" \
  "s_waitcnt vmcnt(4)\n\t" \
  "v_mfma_f32_32x32x16_bf16 v[144:159], v[236:239], v[224:227], v[144:159]\n\t" \
  "v_exp_f32_e32 v49, v49\n\t" \
  "v_exp_f32_e32 v50, v50\n\t" \
  "v_exp_f32_e32 v51, v51\n\t" \
  "buffer_load_dwordx4 v[236:239], v245, %[srd], %[fwo] offen offset:1024 sc1\n\t" \
  "v_smfmac_f32_32x32x32_bf16 v[96:111], v[208:211], v[40:47], v244\n\t" \
  "v_exp_f32_e32 v52, v52\n\t" \
  "v_exp_f32_e32 v53, v53\n\t" \
  "v_mfma_f32_32x32x8_bf16 v[80:95], v[198:199], v[186:187], v[80:95]\n\t" \
  "v_exp_f32_e32 v54, v54\n\t" \
  "v_exp_f32_e32 v55, v55\n\t" \
  "v_exp_f32_e32 v56, v56\n\t" \
  "v_exp_f32_e32 v57, v57\n\t" \
  "s_waitcnt vmcnt(4)\n\t" \
  "v_mfma_f32_32x32x16_bf16 v[160:175], v[240:243], v[224:227], v[160:175]\n\t" \
  "v_exp_f32_e32 v58, v58\n\t" \
  "v_exp_f32_e32 v59, v59\n\t" \
  "v_exp_f32_e32 v60, v60\n\t" \
  "buffer_load_dwordx4 v[240:243], v252, %[srd], %[fwo] offen offset:2048 sc1\n\t" \
  "s_add_u32 %[fwo], %[fwo], 0xc00\n\t" \
  "v_smfmac_f32_32x32x32_bf16 v[112:127], v[200:203], v[48:55], v244\n\t" \
  "v_exp_f32_e32 v61, v61\n\t" \
  "v_exp_f32_e32 v62, v62\n\t" \
  "v_exp_f32_e32 v63, v63\n\t" \
  "s_waitcnt lgkmcnt(0)\n\t" \
  "v_mfma_f32_32x32x16_bf16 v[32:47], v[188:191], v[176:179], 0\n\t" \
  "v_exp_f32_e32 v64, v64\n\t" \
  "v_exp_f32_e32 v65, v65\n\t" \
  "v_exp_f32_e32 v66, v66\n\t" \
  "ds_read_b128 v[192:195], v249 offset:1024\n\t" \
  "ds_read_b64 v[198:199], v250 offset:512\n\t" \
  "s_add_u32 m0, %[dlds], 61440\n\t" \
  "s_sub_u32 %[t0], %[dsrc], 12288\n\t" \
  "buffer_load_dwordx4 v245, %[srd], %[t0] offen sc1 lds\n\t" \
  "buffer_load_dwordx4 v[224:227], v245, %[srd], %[fxo] offen sc1\n\t" \
  "s_add_u32 %[fxo], %[fxo], 0x400\n\t" \
  "v_smfmac_f32_32x32x32_bf16 v[112:127], v[208:211], v[56:63], v244\n\t" \
  "v_exp_f32_e32 v67, v67\n\t" \
  "v_exp_f32_e32 v68, v68\n\t" \
  "ds_read_b128 v[208:211], v251 offset:672\n\t" \
  "v_mfma_f32_32x32x8_bf16 v[32:47], v[196:197], v[184:185], v[32:47]\n\t" \
  "v_exp_f32_e32 v69, v69\n\t" \
  "v_exp_f32_e32 v70, v70\n\t" \
  "v_exp_f32_e32 v71, v71\n\t" \
  "v_exp_f32_e32 v72, v72\n\t" \
  "s_waitcnt vmcnt(4)\n\t" \
  "v_mfma_f32_32x32x16_bf16 v[128:143], v[232:235], v[228:231], v[128:143]\n\t" \
  "v_exp_f32_e32 v73, v73\n\t" \
  "v_exp_f32_e32 v74, v74\n\t" \
  "v_exp_f32_e32 v75, v75\n\t" \
  "buffer_load_dwordx4 v[232:235], v245, %[srd], %[fwo] offen sc1\n\t" \
  "v_smfmac_f32_32x32x32_bf16 v[96:111], v[204:207], v[64:71], v244\n\t" \
  "v_exp_f32_e32 v76, v76\n\t" \
  "v_exp_f32_e32 v77, v77\n\t" \
  "ds_read_b128 v[200:203], v251 offset:0\n\t" \
  "v_mfma_f32_32x32x16_bf16 v[48:63], v[188:191], v[180:183], 0\n\t" \
  "v_exp_f32_e32 v78, v78\n\t" \
  "v_exp_f32_e32 v79, v79\n\t" \
  "v_exp_f32_e32 v80, v80\n\t" \
  "s_waitcnt vmcnt(4)\n\t" \
  "v_mfma_f32_32x32x16_bf16 v[144:159], v[236:239], v[228:231], v[144:159]\n\t" \
  "v_exp_f32_e32 v81, v81\n\t" \
  "v_exp_f32_e32 v82, v82\n\t" \
  "v_exp_f32_e32 v83, v83\n\t" \
  "buffer_load_dwordx4 v[236:239], v245, %[srd], %[fwo] offen offset:1024 sc1\n\t" \
  "v_smfmac_f32_32x32x32_bf16 v[96:111], v[212:215], v[72:79], v244\n\t" \
  "v_exp_f32_e32 v84, v84\n\t" \
  "v_exp_f32_e32 v85, v85\n\t" \
  "v_mfma_f32_32x32x8_bf16 v[48:63], v[196:197], v[186:187], v[48:63]\n\t" \
  "v_exp_f32_e32 v86, v86\n\t" \
  "v_exp_f32_e32 v87, v87\n\t" \
  "v_exp_f32_e32 v88, v88\n\t" \
  "v_exp_f32_e32 v89, v89\n\t" \
  "s_waitcnt vmcnt(4)\n\t" \
  "v_mfma_f32_32x32x16_bf16 v[160:175], v[240:243], v[228:231], v[160:175]\n\t" \
  "v_exp_f32_e32 v90, v90\n\t" \
  "v_exp_f32_e32 v91, v91\n\t" \
  "v_exp_f32_e32 v92, v92\n\t" \
  "buffer_load_dwordx4 v[240:243], v252, %[srd], %[fwo] offen offset:2048 sc1\n\t" \
  "s_add_u32 %[fwo], %[fwo], 0xc00\n\t" \
  "v_smfmac_f32_32x32x32_bf16 v[112:127], v[204:207], v[80:87], v244\n\t" \
  "v_exp_f32_e32 v93, v93\n\t" \
  "v_exp_f32_e32 v94, v94\n\t" \
  "v_exp_f32_e32 v95, v95\n\t" \
  "v_mov_b32_e32 v246, v249\n\t" \
  "v_mov_b32_e32 v247, v250\n\t" \
  "v_mov_b32_e32 v248, v251\n\t" \
  "v_add_u32_e32 v249, 49152, v249\n\t" \
  "v_add_u32_e32 v250, 49152, v250\n\t" \
  "v_add_u32_e32 v251, 49152, v251\n\t" \
  "s_waitcnt lgkmcnt(0)\n\t" \
  "v_mfma_f32_32x32x16_bf16 v[64:79], v[192:195], v[176:179], 0\n\t" \
  "v_exp_f32_e32 v32, v32\n\t" \
  "v_exp_f32_e32 v33, v33\n\t" \
  "v_exp_f32_e32 v34, v34\n\t" \
  "ds_read_b128 v[188:191], v246 offset:2048\n\t" \
  "ds_read_b64 v[196:197], v247 offset:1024\n\t" \
  "s_add_u32 m0, %[dlds], 65536\n\t" \
  "s_sub_u32 %[t0], %[dsrc], 8192\n\t" \
  "buffer_load_dwordx4 v245, %[srd], %[t0] offen sc1 lds\n\t" \
  "buffer_load_dwordx4 v[228:231], v245, %[srd], %[fxo] offen sc1\n\t" \
  "s_add_u32 %[fxo], %[fxo], 0x400\n\t" \
  "v_smfmac_f32_32x32x32_bf16 v[112:127], v[212:215], v[88:95], v244\n\t" \
  "v_exp_f32_e32 v35, v35\n\t" \
  "v_exp_f32_e32 v36, v36\n\t" \
  "ds_read_b128 v[212:215], v248 offset:2016\n\t" \
  "v_mfma_f32_32x32x8_bf16 v[64:79], v[198:199], v[184:185], v[64:79]\n\t" \
  "v_exp_f32_e32 v37, v37\n\t" \
  "v_exp_f32_e32 v38, v38\n\t" \
  "v_exp_f32_e32 v39, v39\n\t" \
  "v_exp_f32_e32 v40, v40\n\t" \
  "s_waitcnt vmcnt(4)\n\t" \
  "v_mfma_f32_32x32x16_bf16 v[128:143], v[232:235], v[216:219], v[128:143]\n\t" \
  "v_exp_f32_e32 v41, v41\n\t" \
  "v_exp_f32_e32 v42, v42\n\t" \
  "v_exp_f32_e32 v43, v43\n\t" \
  "buffer_load_dwordx4 v[232:235], v245, %[srd], %[fwo] offen sc1\n\t" \
  "v_smfmac_f32_32x32x32_bf16 v[96:111], v[200:203], v[32:39], v244\n\t" \
  "v_exp_f32_e32 v44, v44\n\t" \
  "v_exp_f32_e32 v45, v45\n\t" \
  "ds_read_b128 v[204:207], v248 offset:1344\n\t" \
  "v_mfma_f32_32x32x16_bf16 v[80:95], v[192:195], v[180:183], 0\n\t" \
  "v_exp_f32_e32 v46, v46\n\t" \
  "v_exp_f32_e32 v47, v47\n\t" \
  "v_exp_f32_e32 v48, v48\n\t" \
  "s_waitcnt vmcnt(4)\n\t" \
  "v_mfma_f32_32x32x16_bf16 v[144:159], v[236:239], v[216:219], v[144:159]\n\t" \
  "v_exp_f32_e32 v49, v49\n\t" \
  "v_exp_f32_e32 v50, v50\n\t" \
  "v_exp_f32_e32 v51, v51\n\t" \
  "buffer_load_dwordx4 v[236:239], v245, %[srd], %[fwo] offen offset:1024 sc1\n\t" \
  "v_smfmac_f32_32x32x32_bf16 v[96:111], v[208:211], v[40:47], v244\n\t" \
  "v_exp_f32_e32 v52, v52\n\t" \
  "v_exp_f32_e32 v53, v53\n\t" \
  "v_mfma_f32_32x32x8_bf16 v[80:95], v[198:199], v[186:187], v[80:95]\n\t" \
  "v_exp_f32_e32 v54, v54\n\t" \
  "v_exp_f32_e32 v55, v55\n\t" \
  "v_exp_f32_e32 v56, v56\n\t" \
  "v_exp_f32_e32 v57, v57\n\t" \
  "s_waitcnt vmcnt(4)\n\t" \
  "v_mfma_f32_32x32x16_bf16 v[160:175], v[240:243], v[216:219], v[160:175]\n\t" \
  "v_exp_f32_e32 v58, v58\n\t" \
  "v_exp_f32_e32 v59, v59\n\t" \
  "v_exp_f32_e32 v60, v60\n\t" \
  "buffer_load_dwordx4 v[240:243], v252, %[srd], %[fwo] offen offset:2048 sc1\n\t" \
  "s_add_u32 %[fwo], %[fwo], 0xc00\n\t" \
  "v_smfmac_f32_32x32x32_bf16 v[112:127], v[200:203], v[48:55], v244\n\t" \
  "v_exp_f32_e32 v61, v61\n\t" \
  "v_exp_f32_e32 v62, v62\n\t" \
  "v_exp_f32_e32 v63, v63\n\t" \
  "s_waitcnt lgkmcnt(0)\n\t" \
  "v_mfma_f32_32x32x16_bf16 v[32:47], v[188:191], v[176:179], 0\n\t" \
  "v_exp_f32_e32 v64, v64\n\t" \
  "v_exp_f32_e32 v65, v65\n\t" \
  "v_exp_f32_e32 v66, v66\n\t" \
  "ds_read_b128 v[192:195], v246 offset:3072\n\t" \
  "ds_read_b64 v[198:199], v247 offset:1536\n\t" \
  "s_add_u32 m0, %[dlds], 69632\n\t" \
  "s_sub_u32 %[t0], %[dsrc], 4096\n\t" \
  "buffer_load_dwordx4 v245, %[srd], %[t0] offen sc1 lds\n\t" \
  "buffer_load_dwordx4 v[216:219], v245, %[srd], %[fxo] offen sc1\n\t" \
  "s_add_u32 %[fxo], %[fxo], 0x400\n\t" \
  "v_smfmac_f32_32x32x32_bf16 v[112:127], v[208:211], v[56:63], v244\n\t" \
  "v_exp_f32_e32 v67, v67\n\t" \
  "v_exp_f32_e32 v68, v68\n\t" \
  "ds_read_b128 v[208:211], v248 offset:3360\n\t" \
  "v_mfma_f32_32x32x8_bf16 v[32:47], v[196:197], v[184:185], v[32:47]\n\t" \
  "v_exp_f32_e32 v69, v69\n\t" \
  "v_exp_f32_e32 v70, v70\n\t" \
  "v_exp_f32_e32 v71, v71\n\t" \
  "v_exp_f32_e32 v72, v72\n\t" \
  "s_waitcnt vmcnt(4)\n\t" \
  "v_mfma_f32_32x32x16_bf16 v[128:143], v[232:235], v[220:223], v[128:143]\n\t" \
  "v_exp_f32_e32 v73, v73\n\t" \
  "v_exp_f32_e32 v74, v74\n\t" \
  "v_exp_f32_e32 v75, v75\n\t" \
  "buffer_load_dwordx4 v[232:235], v245, %[srd], %[fwo] offen sc1\n\t" \
  "v_smfmac_f32_32x32x32_bf16 v[96:111], v[204:207], v[64:71], v244\n\t" \
  "v_exp_f32_e32 v76, v76\n\t" \
  "v_exp_f32_e32 v77, v77\n\t" \
  "ds_read_b128 v[200:203], v248 offset:2688\n\t" \
  "v_mfma_f32_32x32x16_bf16 v[48:63], v[188:191], v[180:183], 0\n\t" \
  "v_exp_f32_e32 v78, v78\n\t" \
  "v_exp_f32_e32 v79, v79\n\t" \
  "v_exp_f32_e32 v80, v80\n\t" \
  "s_waitcnt vmcnt(4)\n\t" \
  "v_mfma_f32_32x32x16_bf16 v[144:159], v[236:239], v[220:223], v[144:159]\n\t" \
  "v_exp_f32_e32 v81, v81\n\t" \
  "v_exp_f32_e32 v82, v82\n\t" \
  "v_exp_f32_e32 v83, v83\n\t" \
  "buffer_load_dwordx4 v[236:239], v245, %[srd], %[fwo] offen offset:1024 sc1\n\t" \
  "v_smfmac_f32_32x32x32_bf16 v[96:111], v[212:215], v[72:79], v244\n\t" \
  "v_exp_f32_e32 v84, v84\n\t" \
  "v_exp_f32_e32 v85, v85\n\t" \
  "v_mfma_f32_32x32x8_bf16 v[48:63], v[196:197], v[186:187], v[48:63]\n\t" \
  "v_exp_f32_e32 v86, v86\n\t" \
  "v_exp_f32_e32 v87, v87\n\t" \
  "v_exp_f32_e32 v88, v88\n\t" \
  "v_exp_f32_e32 v89, v89\n\t" \
  "s_waitcnt vmcnt(4)\n\t" \
  "v_mfma_f32_32x32x16_bf16 v[160:175], v[240:243], v[220:223], v[160:175]\n\t" \
  "v_exp_f32_e32 v90, v90\n\t" \
  "v_exp_f32_e32 v91, v91\n\t" \
  "v_exp_f32_e32 v92, v92\n\t" \
  "buffer_load_dwordx4 v[240:243], v252, %[srd], %[fwo] offen offset:2048 sc1\n\t" \
  "s_add_u32 %[fwo], %[fwo], 0xc00\n\t" \
  "v_smfmac_f32_32x32x32_bf16 v[112:127], v[204:207], v[80:87], v244\n\t" \
  "v_exp_f32_e32 v93, v93\n\t" \
  "v_exp_f32_e32 v94, v94\n\t" \
  "v_exp_f32_e32 v95, v95\n\t" \
  "s_waitcnt lgkmcnt(0)\n\t" \
  "v_mfma_f32_32x32x16_bf16 v[64:79], v[192:195], v[176:179], 0\n\t" \
  "v_exp_f32_e32 v32, v32\n\t" \
  "v_exp_f32_e32 v33, v33\n\t" \
  "v_exp_f32_e32 v34, v34\n\t" \
  "ds_read_b128 v[188:191], v246 offset:4096\n\t" \
  "ds_read_b64 v[196:197], v247 offset:2048\n\t" \
  "buffer_load_dwordx4 v[220:223], v245, %[srd], %[fxo] offen sc1\n\t" \
  "s_add_u32 %[fxo], %[fxo], 0x400\n\t" \
  "v_smfmac_f32_32x32x32_bf16 v[112:127], v[212:215], v[88:95], v244\n\t" \
  "v_exp_f32_e32 v35, v35\n\t" \
  "v_exp_f32_e32 v36, v36\n\t" \
  "ds_read_b128 v[212:215], v248 offset:4704\n\t" \
  "v_mfma_f32_32x32x8_bf16 v[64:79], v[198:199], v[184:185], v[64:79]\n\t" \
  "v_exp_f32_e32 v37, v37\n\t" \
  "v_exp_f32_e32 v38, v38\n\t" \
  "v_exp_f32_e32 v39, v39\n\t" \
  "v_exp_f32_e32 v40, v40\n\t" \
  "s_waitcnt vmcnt(3)\n\t" \
  "v_mfma_f32_32x32x16_bf16 v[128:143], v[232:235], v[224:227], v[128:143]\n\t" \
  "v_exp_f32_e32 v41, v41\n\t" \
  "v_exp_f32_e32 v42, v42\n\t" \
  "v_exp_f32_e32 v43, v43\n\t" \
  "buffer_load_dwordx4 v[232:235], v245, %[srd], %[fwo] offen sc1\n\t" \
  "v_smfmac_f32_32x32x32_bf16 v[96:111], v[200:203], v[32:39], v244\n\t" \
  "v_exp_f32_e32 v44, v44\n\t" \
  "v_exp_f32_e32 v45, v45\n\t" \
  "ds_read_b128 v[204:207], v248 offset:4032\n\t" \
  "v_mfma_f32_32x32x16_bf16 v[80:95], v[192:195], v[180:183], 0\n\t" \
  "v_exp_f32_e32 v46, v46\n\t" \
  "v_exp_f32_e32 v47, v47\n\t" \
  "v_exp_f32_e32 v48, v48\n\t" \
  "s_waitcnt vmcnt(3)\n\t" \
  "v_mfma_f32_32x32x16_bf16 v[144:159], v[236:239], v[224:227], v[144:159]\n\t" \
  "v_exp_f32_e32 v49, v49\n\t" \
  "v_exp_f32_e32 v50, v50\n\t" \
  "v_exp_f32_e32 v51, v51\n\t" \
  "buffer_load_dwordx4 v[236:239], v245, %[srd], %[fwo] offen offset:1024 sc1\n\t" \
  "v_smfmac_f32_32x32x32_bf16 v[96:111], v[208:211], v[40:47], v244\n\t" \
  "v_exp_f32_e32 v52, v52\n\t" \
  "v_exp_f32_e32 v53, v53\n\t" \
  "v_mfma_f32_32x32x8_bf16 v[80:95], v[198:199], v[186:187], v[80:95]\n\t" \
  "v_exp_f32_e32 v54, v54\n\t" \
  "v_exp_f32_e32 v55, v55\n\t" \
  "v_exp_f32_e32 v56, v56\n\t" \
  "v_exp_f32_e32 v57, v57\n\t" \
  "s_waitcnt vmcnt(3)\n\t" \
  "v_mfma_f32_32x32x16_bf16 v[160:175], v[240:243], v[224:227], v[160:175]\n\t" \
  "v_exp_f32_e32 v58, v58\n\t" \
  "v_exp_f32_e32 v59, v59\n\t" \
  "v_exp_f32_e32 v60, v60\n\t" \
  "buffer_load_dwordx4 v[240:243], v252, %[srd], %[fwo] offen offset:2048 sc1\n\t" \
  "s_add_u32 %[fwo], %[fwo], 0xc00\n\t" \
  "v_smfmac_f32_32x32x32_bf16 v[112:127], v[200:203], v[48:55], v244\n\t" \
  "v_exp_f32_e32 v61, v61\n\t" \
  "v_exp_f32_e32 v62, v62\n\t" \
  "v_exp_f32_e32 v63, v63\n\t" \
  "s_waitcnt lgkmcnt(0)\n\t" \
  "v_mfma_f32_32x32x16_bf16 v[32:47], v[188:191], v[176:179], 0\n\t" \
  "v_exp_f32_e32 v64, v64\n\t" \
  "v_exp_f32_e32 v65, v65\n\t" \
  "v_exp_f32_e32 v66, v66\n\t" \
  "ds_read_b128 v[192:195], v246 offset:5120\n\t" \
  "ds_read_b64 v[198:199], v247 offset:2560\n\t" \
  "buffer_load_dwordx4 v[224:227], v245, %[srd], %[fxo] offen sc1\n\t" \
  "s_add_u32 %[fxo], %[fxo], 0x400\n\t" \
  "v_smfmac_f32_32x32x32_bf16 v[112:127], v[208:211], v[56:63], v244\n\t" \
  "v_exp_f32_e32 v67, v67\n\t" \
  "v_exp_f32_e32 v68, v68\n\t" \
  "ds_read_b128 v[208:211], v248 offset:6048\n\t" \
  "v_mfma_f32_32x32x8_bf16 v[32:47], v[196:197], v[184:185], v[32:47]\n\t" \
  "v_exp_f32_e32 v69, v69\n\t" \
  "v_exp_f32_e32 v70, v70\n\t" \
  "v_exp_f32_e32 v71, v71\n\t" \
  "v_exp_f32_e32 v72, v72\n\t" \
  "s_waitcnt vmcnt(3)\n\t" \
  "v_mfma_f32_32x32x16_bf16 v[128:143], v[232:235], v[228:231], v[128:143]\n\t" \
  "v_exp_f32_e32 v73, v73\n\t" \
  "v_exp_f32_e32 v74, v74\n\t" \
  "v_exp_f32_e32 v75, v75\n\t" \
  "buffer_load_dwordx4 v[232:235], v245, %[srd], %[fwo] offen sc1\n\t" \
  "v_smfmac_f32_32x32x32_bf16 v[96:111], v[204:207], v[64:71], v244\n\t" \
  "v_exp_f32_e32 v76, v76\n\t" \
  "v_exp_f32_e32 v77, v77\n\t" \
  "ds_read_b128 v[200:203], v248 offset:5376\n\t" \
  "v_mfma_f32_32x32x16_bf16 v[48:63], v[188:191], v[180:183], 0\n\t" \
  "v_exp_f32_e32 v78, v78\n\t" \
  "v_exp_f32_e32 v79, v79\n\t" \
  "v_exp_f32_e32 v80, v80\n\t" \
  "s_waitcnt vmcnt(3)\n\t" \
  "v_mfma_f32_32x32x16_bf16 v[144:159], v[236:239], v[228:231], v[144:159]\n\t" \
  "v_exp_f32_e32 v81, v81\n\t" \
  "v_exp_f32_e32 v82, v82\n\t" \
  "v_exp_f32_e32 v83, v83\n\t" \
  "buffer_load_dwordx4 v[236:239], v245, %[srd], %[fwo] offen offset:1024 sc1\n\t" \
  "v_smfmac_f32_32x32x32_bf16 v[96:111], v[212:215], v[72:79], v244\n\t" \
  "v_exp_f32_e32 v84, v84\n\t" \
  "v_exp_f32_e32 v85, v85\n\t" \
  "v_mfma_f32_32x32x8_bf16 v[48:63], v[196:197], v[186:187], v[48:63]\n\t" \
  "v_exp_f32_e32 v86, v86\n\t" \
  "v_exp_f32_e32 v87, v87\n\t" \
  "v_exp_f32_e32 v88, v88\n\t" \
  "v_exp_f32_e32 v89, v89\n\t" \
  "s_waitcnt vmcnt(3)\n\t" \
  "v_mfma_f32_32x32x16_bf16 v[160:175], v[240:243], v[228:231], v[160:175]\n\t" \
  "v_exp_f32_e32 v90, v90\n\t" \
  "v_exp_f32_e32 v91, v91\n\t" \
  "v_exp_f32_e32 v92, v92\n\t" \
  "buffer_load_dwordx4 v[240:243], v252, %[srd], %[fwo] offen offset:2048 sc1\n\t" \
  "s_add_u32 %[fwo], %[fwo], 0xc00\n\t" \
  "v_smfmac_f32_32x32x32_bf16 v[112:127], v[204:207], v[80:87], v244\n\t" \
  "v_exp_f32_e32 v93, v93\n\t" \
  "v_exp_f32_e32 v94, v94\n\t" \
  "v_exp_f32_e32 v95, v95\n\t" \
  "s_waitcnt lgkmcnt(0)\n\t" \
  "v_mfma_f32_32x32x16_bf16 v[64:79], v[192:195], v[176:179], 0\n\t" \
  "v_exp_f32_e32 v32, v32\n\t" \
  "v_exp_f32_e32 v33, v33\n\t" \
  "v_exp_f32_e32 v34, v34\n\t" \
  "ds_read_b128 v[188:191], v246 offset:6144\n\t" \
  "ds_read_b64 v[196:197], v247 offset:3072\n\t" \
  "buffer_load_dwordx4 v[228:231], v245, %[srd], %[fxo] offen sc1\n\t" \
  "s_add_u32 %[fxo], %[fxo], 0x400\n\t" \
  "v_smfmac_f32_32x32x32_bf16 v[112:127], v[212:215], v[88:95], v244\n\t" \
  "v_exp_f32_e32 v35, v35\n\t" \
  "v_exp_f32_e32 v36, v36\n\t" \
  "ds_read_b128 v[212:215], v248 offset:7392\n\t" \
  "v_mfma_f32_32x32x8_bf16 v[64:79], v[198:199], v[184:185], v[64:79]\n\t" \
  "v_exp_f32_e32 v37, v37\n\t" \
  "v_exp_f32_e32 v38, v38\n\t" \
  "v_exp_f32_e32 v39, v39\n\t" \
  "v_exp_f32_e32 v40, v40\n\t" \
  "s_waitcnt vmcnt(3)\n\t" \
  "v_mfma_f32_32x32x16_bf16 v[128:143], v[232:235], v[216:219], v[128:143]\n\t" \
  "v_exp_f32_e32 v41, v41\n\t" \
  "v_exp_f32_e32 v42, v42\n\t" \
  "v_exp_f32_e32 v43, v43\n\t" \
  "buffer_load_dwordx4 v[232:235], v245, %[srd], %[fwo] offen sc1\n\t" \
  "v_smfmac_f32_32x32x32_bf16 v[96:111], v[200:203], v[32:39], v244\n\t" \
  "v_exp_f32_e32 v44, v44\n\t" \
  "v_exp_f32_e32 v45, v45\n\t" \
  "ds_read_b128 v[204:207], v248 offset:6720\n\t" \
  "v_mfma_f32_32x32x16_bf16 v[80:95], v[192:195], v[180:183], 0\n\t" \
  "v_exp_f32_e32 v46, v46\n\t" \
  "v_exp_f32_e32 v47, v47\n\t" \
  "v_exp_f32_e32 v48, v48\n\t" \
  "s_waitcnt vmcnt(3)\n\t" \
  "v_mfma_f32_32x32x16_bf16 v[144:159], v[236:239], v[216:219], v[144:159]\n\t" \
  "v_exp_f32_e32 v49, v49\n\t" \
  "v_exp_f32_e32 v50, v50\n\t" \
  "v_exp_f32_e32 v51, v51\n\t" \
  "buffer_load_dwordx4 v[236:239], v245, %[srd], %[fwo] offen offset:1024 sc1\n\t" \
  "v_smfmac_f32_32x32x32_bf16 v[96:111], v[208:211], v[40:47], v244\n\t" \
  "v_exp_f32_e32 v52, v52\n\t" \
  "v_exp_f32_e32 v53, v53\n\t" \
  "v_mfma_f32_32x32x8_bf16 v[80:95], v[198:199], v[186:187], v[80:95]\n\t" \
  "v_exp_f32_e32 v54, v54\n\t" \
  "v_exp_f32_e32 v55, v55\n\t" \
  "v_exp_f32_e32 v56, v56\n\t" \
  "v_exp_f32_e32 v57, v57\n\t" \
  "s_waitcnt vmcnt(3)\n\t" \
  "v_mfma_f32_32x32x16_bf16 v[160:175], v[240:243], v[216:219], v[160:175]\n\t" \
  "v_exp_f32_e32 v58, v58\n\t" \
  "v_exp_f32_e32 v59, v59\n\t" \
  "v_exp_f32_e32 v60, v60\n\t" \
  "buffer_load_dwordx4 v[240:243], v252, %[srd], %[fwo] offen offset:2048 sc1\n\t" \
  "s_add_u32 %[fwo], %[fwo], 0xc00\n\t" \
  "v_smfmac_f32_32x32x32_bf16 v[112:127], v[200:203], v[48:55], v244\n\t" \
  "v_exp_f32_e32 v61, v61\n\t" \
  "v_exp_f32_e32 v62, v62\n\t" \
  "v_exp_f32_e32 v63, v63\n\t" \
  "s_waitcnt lgkmcnt(0)\n\t" \
  "v_mfma_f32_32x32x16_bf16 v[32:47], v[188:191], v[176:179], 0\n\t" \
  "v_exp_f32_e32 v64, v64\n\t" \
  "v_exp_f32_e32 v65, v65\n\t" \
  "v_exp_f32_e32 v66, v66\n\t" \
  "ds_read_b128 v[192:195], v246 offset:7168\n\t" \
  "ds_read_b64 v[198:199], v247 offset:3584\n\t" \
  "buffer_load_dwordx4 v[216:219], v245, %[srd], %[fxo] offen sc1\n\t" \
  "s_add_u32 %[fxo], %[fxo], 0x400\n\t" \
  "v_smfmac_f32_32x32x32_bf16 v[112:127], v[208:211], v[56:63], v244\n\t" \
  "v_exp_f32_e32 v67, v67\n\t" \
  "v_exp_f32_e32 v68, v68\n\t" \
  "ds_read_b128 v[208:211], v248 offset:8736\n\t" \
  "v_mfma_f32_32x32x8_bf16 v[32:47], v[196:197], v[184:185], v[32:47]\n\t" \
  "v_exp_f32_e32 v69, v69\n\t" \
  "v_exp_f32_e32 v70, v70\n\t" \
  "v_exp_f32_e32 v71, v71\n\t" \
  "v_exp_f32_e32 v72, v72\n\t" \
  "s_waitcnt vmcnt(3)\n\t" \
  "v_mfma_f32_32x32x16_bf16 v[128:143], v[232:235], v[220:223], v[128:143]\n\t" \
  "v_exp_f32_e32 v73, v73\n\t" \
  "v_exp_f32_e32 v74, v74\n\t" \
  "v_exp_f32_e32 v75, v75\n\t" \
  "buffer_load_dwordx4 v[232:235], v245, %[srd], %[fwo] offen sc1\n\t" \
  "v_smfmac_f32_32x32x32_bf16 v[96:111], v[204:207], v[64:71], v244\n\t" \
  "v_exp_f32_e32 v76, v76\n\t" \
  "v_exp_f32_e32 v77, v77\n\t" \
  "ds_read_b128 v[200:203], v248 offset:8064\n\t" \
  "v_mfma_f32_32x32x16_bf16 v[48:63], v[188:191], v[180:183], 0\n\t" \
  "v_exp_f32_e32 v78, v78\n\t" \
  "v_exp_f32_e32 v79, v79\n\t" \
  "v_exp_f32_e32 v80, v80\n\t" \
  "s_waitcnt vmcnt(3)\n\t" \
  "v_mfma_f32_32x32x16_bf16 v[144:159], v[236:239], v[220:223], v[144:159]\n\t" \
  "v_exp_f32_e32 v81, v81\n\t" \
  "v_exp_f32_e32 v82, v82\n\t" \
  "v_exp_f32_e32 v83, v83\n\t" \
  "buffer_load_dwordx4 v[236:239], v245, %[srd], %[fwo] offen offset:1024 sc1\n\t" \
  "v_smfmac_f32_32x32x32_bf16 v[96:111], v[212:215], v[72:79], v244\n\t" \
  "v_exp_f32_e32 v84, v84\n\t" \
  "v_exp_f32_e32 v85, v85\n\t" \
  "v_mfma_f32_32x32x8_bf16 v[48:63], v[196:197], v[186:187], v[48:63]\n\t" \
  "v_exp_f32_e32 v86, v86\n\t" \
  "v_exp_f32_e32 v87, v87\n\t" \
  "v_exp_f32_e32 v88, v88\n\t" \
  "v_exp_f32_e32 v89, v89\n\t" \
  "s_waitcnt vmcnt(3)\n\t" \
  "v_mfma_f32_32x32x16_bf16 v[160:175], v[240:243], v[220:223], v[160:175]\n\t" \
  "v_exp_f32_e32 v90, v90\n\t" \
  "v_exp_f32_e32 v91, v91\n\t" \
  "v_exp_f32_e32 v92, v92\n\t" \
  "buffer_load_dwordx4 v[240:243], v252, %[srd], %[fwo] offen offset:2048 sc1\n\t" \
  "s_add_u32 %[fwo], %[fwo], 0xc00\n\t" \
  "v_smfmac_f32_32x32x32_bf16 v[112:127], v[204:207], v[80:87], v244\n\t" \
  "v_exp_f32_e32 v93, v93\n\t" \
  "v_exp_f32_e32 v94, v94\n\t" \
  "v_exp_f32_e32 v95, v95\n\t" \
  "s_waitcnt lgkmcnt(0)\n\t" \
  "s_barrier\n\t" \
  "v_mfma_f32_32x32x16_bf16 v[64:79], v[192:195], v[176:179], 0\n\t" \
  "v_exp_f32_e32 v32, v32\n\t" \
  "v_exp_f32_e32 v33, v33\n\t" \
  "v_exp_f32_e32 v34, v34\n\t" \
  "ds_read_b128 v[188:191], v249 offset:0\n\t" \
  "ds_read_b64 v[196:197], v250 offset:0\n\t" \
  "s_add_u32 m0, %[dlds], 98304\n\t" \
  "s_add_u32 %[t0], %[dsrc], 0\n\t" \
  "buffer_load_dwordx4 v245, %[srd], %[t0] offen sc1 lds\n\t" \
  "buffer_load_dwordx4 v[220:223], v245, %[srd], %[fxo] offen sc1\n\t" \
  "s_add_u32 %[fxo], %[fxo], 0x400\n\t" \
  "v_smfmac_f32_32x32x32_bf16 v[112:127], v[212:215], v[88:95], v244\n\t" \
  "v_exp_f32_e32 v35, v35\n\t" \
  "v_exp_f32_e32 v36, v36\n\t" \
  "ds_read_b128 v[212:215], v248 offset:10080\n\t" \
  "v_mfma_f32_32x32x8_bf16 v[64:79], v[198:199], v[184:185], v[64:79]\n\t" \
  "v_exp_f32_e32 v37, v37\n\t" \
  "v_exp_f32_e32 v38, v38\n\t" \
  "v_exp_f32_e32 v39, v39\n\t" \
  "v_exp_f32_e32 v40, v40\n\t" \
  "s_waitcnt vmcnt(4)\n\t" \
  "v_mfma_f32_32x32x16_bf16 v[128:143], v[232:235], v[224:227], v[128:143]\n\t" \
  "v_exp_f32_e32 v41, v41\n\t" \
  "v_exp_f32_e32 v42, v42\n\t" \
  "v_exp_f32_e32 v43, v43\n\t" \
  "buffer_load_dwordx4 v[232:235], v245, %[srd], %[fwo] offen sc1\n\t" \
  "v_smfmac_f32_32x32x32_bf16 v[96:111], v[200:203], v[32:39], v244\n\t" \
  "v_exp_f32_e32 v44, v44\n\t" \
  "v_exp_f32_e32 v45, v45\n\t" \
  "ds_read_b128 v[204:207], v248 offset:9408\n\t" \
  "v_mfma_f32_32x32x16_bf16 v[80:95], v[192:195], v[180:183], 0\n\t" \
  "v_exp_f32_e32 v46, v46\n\t" \
  "v_exp_f32_e32 v47, v47\n\t" \
  "v_exp_f32_e32 v48, v48\n\t" \
  "s_waitcnt vmcnt(4)\n\t" \
  "v_mfma_f32_32x32x16_bf16 v[144:159], v[236:239], v[224:227], v[144:159]\n\t" \
  "v_exp_f32_e32 v49, v49\n\t" \
  "v_exp_f32_e32 v50, v50\n\t" \
  "v_exp_f32_e32 v51, v51\n\t" \
  "buffer_load_dwordx4 v[236:239], v245, %[srd], %[fwo] offen offset:1024 sc1\n\t" \
  "v_smfmac_f32_32x32x32_bf16 v[96:111], v[208:211], v[40:47], v244\n\t" \
  "v_exp_f32_e32 v52, v52\n\t" \
  "v_exp_f32_e32 v53, v53\n\t" \
  "v_mfma_f32_32x32x8_bf16 v[80:95], v[198:199], v[186:187], v[80:95]\n\t" \
  "v_exp_f32_e32 v54, v54\n\t" \
  "v_exp_f32_e32 v55, v55\n\t" \
  "v_exp_f32_e32 v56, v56\n\t" \
  "v_exp_f32_e32 v57, v57\n\t" \
  "s_waitcnt vmcnt(4)\n\t" \
  "v_mfma_f32_32x32x16_bf16 v[160:175], v[240:243], v[224:227], v[160:175]\n\t" \
  "v_exp_f32_e32 v58, v58\n\t" \
  "v_exp_f32_e32 v59, v59\n\t" \
  "v_exp_f32_e32 v60, v60\n\t" \
  "buffer_load_dwordx4 v[240:243], v252, %[srd], %[fwo] offen offset:2048 sc1\n\t" \
  "s_add_u32 %[fwo], %[fwo], 0xc00\n\t" \
  "v_smfmac_f32_32x32x32_bf16 v[112:127], v[200:203], v[48:55], v244\n\t" \
  "v_exp_f32_e32 v61, v61\n\t" \
  "v_exp_f32_e32 v62, v62\n\t" \
  "v_exp_f32_e32 v63, v63\n\t" \
  "s_waitcnt lgkmcnt(0)\n\t" \
  "v_mfma_f32_32x32x16_bf16 v[32:47], v[188:191], v[176:179], 0\n\t" \
  "v_exp_f32_e32 v64, v64\n\t" \
  "v_exp_f32_e32 v65, v65\n\t" \
  "v_exp_f32_e32 v66, v66\n\t" \
  "ds_read_b128 v[192:195], v249 offset:1024\n\t" \
  "ds_read_b64 v[198:199], v250 offset:512\n\t" \
  "s_add_u32 m0, %[dlds], 102400\n\t" \
  "s_add_u32 %[t0], %[dsrc], 4096\n\t" \
  "buffer_load_dwordx4 v245, %[srd], %[t0] offen sc1 lds\n\t" \
  "buffer_load_dwordx4 v[224:227], v245, %[srd], %[fxo] offen sc1\n\t" \
  "s_add_u32 %[fxo], %[fxo], 0x400\n\t" \
  "v_smfmac_f32_32x32x32_bf16 v[112:127], v[208:211], v[56:63], v244\n\t" \
  "v_exp_f32_e32 v67, v67\n\t" \
  "v_exp_f32_e32 v68, v68\n\t" \
  "ds_read_b128 v[208:211], v251 offset:672\n\t" \
  "v_mfma_f32_32x32x8_bf16 v[32:47], v[196:197], v[184:185], v[32:47]\n\t" \
  "v_exp_f32_e32 v69, v69\n\t" \
  "v_exp_f32_e32 v70, v70\n\t" \
  "v_exp_f32_e32 v71, v71\n\t" \
  "v_exp_f32_e32 v72, v72\n\t" \
  "s_waitcnt vmcnt(4)\n\t" \
  "v_mfma_f32_32x32x16_bf16 v[128:143], v[232:235], v[228:231], v[128:143]\n\t" \
  "v_exp_f32_e32 v73, v73\n\t" \
  "v_exp_f32_e32 v74, v74\n\t" \
  "v_exp_f32_e32 v75, v75\n\t" \
  "buffer_load_dwordx4 v[232:235], v245, %[srd], %[fwo] offen sc1\n\t" \
  "v_smfmac_f32_32x32x32_bf16 v[96:111], v[204:207], v[64:71], v244\n\t" \
  "v_exp_f32_e32 v76, v76\n\t" \
  "v_exp_f32_e32 v77, v77\n\t" \
  "ds_read_b128 v[200:203], v251 offset:0\n\t" \
  "v_mfma_f32_32x32x16_bf16 v[48:63], v[188:191], v[180:183], 0\n\t" \
  "v_exp_f32_e32 v78, v78\n\t" \
  "v_exp_f32_e32 v79, v79\n\t" \
  "v_exp_f32_e32 v80, v80\n\t" \
  "s_waitcnt vmcnt(4)\n\t" \
  "v_mfma_f32_32x32x16_bf16 v[144:159], v[236:239], v[228:231], v[144:159]\n\t" \
  "v_exp_f32_e32 v81, v81\n\t" \
  "v_exp_f32_e32 v82, v82\n\t" \
  "v_exp_f32_e32 v83, v83\n\t" \
  "buffer_load_dwordx4 v[236:239], v245, %[srd], %[fwo] offen offset:1024 sc1\n\t" \
  "v_smfmac_f32_32x32x32_bf16 v[96:111], v[212:215], v[72:79], v244\n\t" \
  "v_exp_f32_e32 v84, v84\n\t" \
  "v_exp_f32_e32 v85, v85\n\t" \
  "v_mfma_f32_32x32x8_bf16 v[48:63], v[196:197], v[186:187], v[48:63]\n\t" \
  "v_exp_f32_e32 v86, v86\n\t" \
  "v_exp_f32_e32 v87, v87\n\t" \
  "v_exp_f32_e32 v88, v88\n\t" \
  "v_exp_f32_e32 v89, v89\n\t" \
  "s_waitcnt vmcnt(4)\n\t" \
  "v_mfma_f32_32x32x16_bf16 v[160:175], v[240:243], v[228:231], v[160:175]\n\t" \
  "v_exp_f32_e32 v90, v90\n\t" \
  "v_exp_f32_e32 v91, v91\n\t" \
  "v_exp_f32_e32 v92, v92\n\t" \
  "buffer_load_dwordx4 v[240:243], v252, %[srd], %[fwo] offen offset:2048 sc1\n\t" \
  "s_add_u32 %[fwo], %[fwo], 0xc00\n\t" \
  "v_smfmac_f32_32x32x32_bf16 v[112:127], v[204:207], v[80:87], v244\n\t" \
  "v_exp_f32_e32 v93, v93\n\t" \
  "v_exp_f32_e32 v94, v94\n\t" \
  "v_exp_f32_e32 v95, v95\n\t" \
  "v_mov_b32_e32 v246, v249\n\t" \
  "v_mov_b32_e32 v247, v250\n\t" \
  "v_mov_b32_e32 v248, v251\n\t" \
  "v_add_u32_e32 v249, 49152, v249\n\t" \
  "v_add_u32_e32 v250, 49152, v250\n\t" \
  "v_add_u32_e32 v251, 49152, v251\n\t" \
  "s_waitcnt lgkmcnt(0)\n\t" \
  "v_mfma_f32_32x32x16_bf16 v[64:79], v[192:195], v[176:179], 0\n\t" \
  "v_exp_f32_e32 v32, v32\n\t" \
  "v_exp_f32_e32 v33, v33\n\t" \
  "v_exp_f32_e32 v34, v34\n\t" \
  "ds_read_b128 v[188:191], v246 offset:2048\n\t" \
  "ds_read_b64 v[196:197], v247 offset:1024\n\t" \
  "s_add_u32 m0, %[dlds], 106496\n\t" \
  "s_add_u32 %[t0], %[dsrc], 8192\n\t" \
  "buffer_load_dwordx4 v245, %[srd], %[t0] offen sc1 lds\n\t" \
  "buffer_load_dwordx4 v[228:231], v245, %[srd], %[fxo] offen sc1\n\t" \
  "s_add_u32 %[fxo], %[fxo], 0x400\n\t" \
  "v_smfmac_f32_32x32x32_bf16 v[112:127], v[212:215], v[88:95], v244\n\t" \
  "v_exp_f32_e32 v35, v35\n\t" \
  "v_exp_f32_e32 v36, v36\n\t" \
  "ds_read_b128 v[212:215], v248 offset:2016\n\t" \
  "v_mfma_f32_32x32x8_bf16 v[64:79], v[198:199], v[184:185], v[64:79]\n\t" \
  "v_exp_f32_e32 v37, v37\n\t" \
  "v_exp_f32_e32 v38, v38\n\t" \
  "v_exp_f32_e32 v39, v39\n\t" \
  "v_exp_f32_e32 v40, v40\n\t" \
  "s_waitcnt vmcnt(4)\n\t" \
  "v_mfma_f32_32x32x16_bf16 v[128:143], v[232:235], v[216:219], v[128:143]\n\t" \
  "v_exp_f32_e32 v41, v41\n\t" \
  "v_exp_f32_e32 v42, v42\n\t" \
  "v_exp_f32_e32 v43, v43\n\t" \
  "buffer_load_dwordx4 v[232:235], v245, %[srd], %[fwo] offen sc1\n\t" \
  "v_smfmac_f32_32x32x32_bf16 v[96:111], v[200:203], v[32:39], v244\n\t" \
  "v_exp_f32_e32 v44, v44\n\t" \
  "v_exp_f32_e32 v45, v45\n\t" \
  "ds_read_b128 v[204:207], v248 offset:1344\n\t" \
  "v_mfma_f32_32x32x16_bf16 v[80:95], v[192:195], v[180:183], 0\n\t" \
  "v_exp_f32_e32 v46, v46\n\t" \
  "v_exp_f32_e32 v47, v47\n\t" \
  "v_exp_f32_e32 v48, v48\n\t" \
  "s_waitcnt vmcnt(4)\n\t" \
  "v_mfma_f32_32x32x16_bf16 v[144:159], v[236:239], v[216:219], v[144:159]\n\t" \
  "v_exp_f32_e32 v49, v49\n\t" \
  "v_exp_f32_e32 v50, v50\n\t" \
  "v_exp_f32_e32 v51, v51\n\t" \
  "buffer_load_dwordx4 v[236:239], v245, %[srd], %[fwo] offen offset:1024 sc1\n\t" \
  "v_smfmac_f32_32x32x32_bf16 v[96:111], v[208:211], v[40:47], v244\n\t" \
  "v_exp_f32_e32 v52, v52\n\t" \
  "v_exp_f32_e32 v53, v53\n\t" \
  "v_mfma_f32_32x32x8_bf16 v[80:95], v[198:199], v[186:187], v[80:95]\n\t" \
  "v_exp_f32_e32 v54, v54\n\t" \
  "v_exp_f32_e32 v55, v55\n\t" \
  "v_exp_f32_e32 v56, v56\n\t" \
  "v_exp_f32_e32 v57, v57\n\t" \
  "s_waitcnt vmcnt(4)\n\t" \
  "v_mfma_f32_32x32x16_bf16 v[160:175], v[240:243], v[216:219], v[160:175]\n\t" \
  "v_exp_f32_e32 v58, v58\n\t" \
  "v_exp_f32_e32 v59, v59\n\t" \
  "v_exp_f32_e32 v60, v60\n\t" \
  "buffer_load_dwordx4 v[240:243], v252, %[srd], %[fwo] offen offset:2048 sc1\n\t" \
  "s_add_u32 %[fwo], %[fwo], 0xc00\n\t" \
  "v_smfmac_f32_32x32x32_bf16 v[112:127], v[200:203], v[48:55], v244\n\t" \
  "v_exp_f32_e32 v61, v61\n\t" \
  "v_exp_f32_e32 v62, v62\n\t" \
  "v_exp_f32_e32 v63, v63\n\t" \
  "s_waitcnt lgkmcnt(0)\n\t" \
  "v_mfma_f32_32x32x16_bf16 v[32:47], v[188:191], v[176:179], 0\n\t" \
  "v_exp_f32_e32 v64, v64\n\t" \
  "v_exp_f32_e32 v65, v65\n\t" \
  "v_exp_f32_e32 v66, v66\n\t" \
  "ds_read_b128 v[192:195], v246 offset:3072\n\t" \
  "ds_read_b64 v[198:199], v247 offset:1536\n\t" \
  "s_add_u32 m0, %[dlds], 110592\n\t" \
  "s_add_u32 %[t0], %[dsrc], 12288\n\t" \
  "buffer_load_dwordx4 v245, %[srd], %[t0] offen sc1 lds\n\t" \
  "buffer_load_dwordx4 v[216:219], v245, %[srd], %[fxo] offen sc1\n\t" \
  "s_add_u32 %[fxo], %[fxo], 0x400\n\t" \
  "v_smfmac_f32_32x32x32_bf16 v[112:127], v[208:211], v[56:63], v244\n\t" \
  "v_exp_f32_e32 v67, v67\n\t" \
  "v_exp_f32_e32 v68, v68\n\t" \
  "ds_read_b128 v[208:211], v248 offset:3360\n\t" \
  "v_mfma_f32_32x32x8_bf16 v[32:47], v[196:197], v[184:185], v[32:47]\n\t" \
  "v_exp_f32_e32 v69, v69\n\t" \
  "v_exp_f32_e32 v70, v70\n\t" \
  "v_exp_f32_e32 v71, v71\n\t" \
  "v_exp_f32_e32 v72, v72\n\t" \
  "s_waitcnt vmcnt(4)\n\t" \
  "v_mfma_f32_32x32x16_bf16 v[128:143], v[232:235], v[220:223], v[128:143]\n\t" \
  "v_exp_f32_e32 v73, v73\n\t" \
  "v_exp_f32_e32 v74, v74\n\t" \
  "v_exp_f32_e32 v75, v75\n\t" \
  "buffer_load_dwordx4 v[232:235], v245, %[srd], %[fwo] offen sc1\n\t" \
  "v_smfmac_f32_32x32x32_bf16 v[96:111], v[204:207], v[64:71], v244\n\t" \
  "v_exp_f32_e32 v76, v76\n\t" \
  "v_exp_f32_e32 v77, v77\n\t" \
  "ds_read_b128 v[200:203], v248 offset:2688\n\t" \
  "v_mfma_f32_32x32x16_bf16 v[48:63], v[188:191], v[180:183], 0\n\t" \
  "v_exp_f32_e32 v78, v78\n\t" \
  "v_exp_f32_e32 v79, v79\n\t" \
  "v_exp_f32_e32 v80, v80\n\t" \
  "s_waitcnt vmcnt(4)\n\t" \
  "v_mfma_f32_32x32x16_bf16 v[144:159], v[236:239], v[220:223], v[144:159]\n\t" \
  "v_exp_f32_e32 v81, v81\n\t" \
  "v_exp_f32_e32 v82, v82\n\t" \
  "v_exp_f32_e32 v83, v83\n\t" \
  "buffer_load_dwordx4 v[236:239], v245, %[srd], %[fwo] offen offset:1024 sc1\n\t" \
  "v_smfmac_f32_32x32x32_bf16 v[96:111], v[212:215], v[72:79], v244\n\t" \
  "v_exp_f32_e32 v84, v84\n\t" \
  "v_exp_f32_e32 v85, v85\n\t" \
  "v_mfma_f32_32x32x8_bf16 v[48:63], v[196:197], v[186:187], v[48:63]\n\t" \
  "v_exp_f32_e32 v86, v86\n\t" \
  "v_exp_f32_e32 v87, v87\n\t" \
  "v_exp_f32_e32 v88, v88\n\t" \
  "v_exp_f32_e32 v89, v89\n\t" \
  "s_waitcnt vmcnt(4)\n\t" \
  "v_mfma_f32_32x32x16_bf16 v[160:175], v[240:243], v[220:223], v[160:175]\n\t" \
  "v_exp_f32_e32 v90, v90\n\t" \
  "v_exp_f32_e32 v91, v91\n\t" \
  "v_exp_f32_e32 v92, v92\n\t" \
  "buffer_load_dwordx4 v[240:243], v252, %[srd], %[fwo] offen offset:2048 sc1\n\t" \
  "s_add_u32 %[fwo], %[fwo], 0xc00\n\t" \
  "v_smfmac_f32_32x32x32_bf16 v[112:127], v[204:207], v[80:87], v244\n\t" \
  "v_exp_f32_e32 v93, v93\n\t" \
  "v_exp_f32_e32 v94, v94\n\t" \
  "v_exp_f32_e32 v95, v95\n\t" \
  "s_waitcnt lgkmcnt(0)\n\t" \
  "v_mfma_f32_32x32x16_bf16 v[64:79], v[192:195], v[176:179], 0\n\t" \
  "v_exp_f32_e32 v32, v32\n\t" \
  "v_exp_f32_e32 v33, v33\n\t" \
  "v_exp_f32_e32 v34, v34\n\t" \
  "ds_read_b128 v[188:191], v246 offset:4096\n\t" \
  "ds_read_b64 v[196:197], v247 offset:2048\n\t" \
  "s_add_u32 m0, %[dlds], 114688\n\t" \
  "s_add_u32 %[t0], %[dsrc], 16384\n\t" \
  "buffer_load_dwordx4 v245, %[srd], %[t0] offen sc1 lds\n\t" \
  "buffer_load_dwordx4 v[220:223], v245, %[srd], %[fxo] offen sc1\n\t" \
  "s_add_u32 %[fxo], %[fxo], 0x400\n\t" \
  "v_smfmac_f32_32x32x32_bf16 v[112:127], v[212:215], v[88:95], v244\n\t" \
  "v_exp_f32_e32 v35, v35\n\t" \
  "v_exp_f32_e32 v36, v36\n\t" \
  "ds_read_b128 v[212:215], v248 offset:4704\n\t" \
  "v_mfma_f32_32x32x8_bf16 v[64:79], v[198:199], v[184:185], v[64:79]\n\t" \
  "v_exp_f32_e32 v37, v37\n\t" \
  "v_exp_f32_e32 v38, v38\n\t" \
  "v_exp_f32_e32 v39, v39\n\t" \
  "v_exp_f32_e32 v40, v40\n\t" \
  "s_waitcnt vmcnt(4)\n\t" \
  "v_mfma_f32_32x32x16_bf16 v[128:143], v[232:235], v[224:227], v[128:143]\n\t" \
  "v_exp_f32_e32 v41, v41\n\t" \
  "v_exp_f32_e32 v42, v42\n\t" \
  "v_exp_f32_e32 v43, v43\n\t" \
  "buffer_load_dwordx4 v[232:235], v245, %[srd], %[fwo] offen sc1\n\t" \
  "v_smfmac_f32_32x32x32_bf16 v[96:111], v[200:203], v[32:39], v244\n\t" \
  "v_exp_f32_e32 v44, v44\n\t" \
  "v_exp_f32_e32 v45, v45\n\t" \
  "ds_read_b128 v[204:207], v248 offset:4032\n\t" \
  "v_mfma_f32_32x32x16_bf16 v[80:95], v[192:195], v[180:183], 0\n\t" \
  "v_exp_f32_e32 v46, v46\n\t" \
  "v_exp_f32_e32 v47, v47\n\t" \
  "v_exp_f32_e32 v48, v48\n\t" \
  "s_waitcnt vmcnt(4)\n\t" \
  "v_mfma_f32_32x32x16_bf16 v[144:159], v[236:239], v[224:227], v[144:159]\n\t" \
  "v_exp_f32_e32 v49, v49\n\t" \
  "v_exp_f32_e32 v50, v50\n\t" \
  "v_exp_f32_e32 v51, v51\n\t" \
  "buffer_load_dwordx4 v[236:239], v245, %[srd], %[fwo] offen offset:1024 sc1\n\t" \
  "v_smfmac_f32_32x32x32_bf16 v[96:111], v[208:211], v[40:47], v244\n\t" \
  "v_exp_f32_e32 v52, v52\n\t" \
  "v_exp_f32_e32 v53, v53\n\t" \
  "v_mfma_f32_32x32x8_bf16 v[80:95], v[198:199], v[186:187], v[80:95]\n\t" \
  "v_exp_f32_e32 v54, v54\n\t" \
  "v_exp_f32_e32 v55, v55\n\t" \
  "v_exp_f32_e32 v56, v56\n\t" \
  "v_exp_f32_e32 v57, v57\n\t" \
  "s_waitcnt vmcnt(4)\n\t" \
  "v_mfma_f32_32x32x16_bf16 v[160:175], v[240:243], v[224:227], v[160:175]\n\t" \
  "v_exp_f32_e32 v58, v58\n\t" \
  "v_exp_f32_e32 v59, v59\n\t" \
  "v_exp_f32_e32 v60, v60\n\t" \
  "buffer_load_dwordx4 v[240:243], v252, %[srd], %[fwo] offen offset:2048 sc1\n\t" \
  "s_add_u32 %[fwo], %[fwo], 0xc00\n\t" \
  "v_smfmac_f32_32x32x32_bf16 v[112:127], v[200:203], v[48:55], v244\n\t" \
  "v_exp_f32_e32 v61, v61\n\t" \
  "v_exp_f32_e32 v62, v62\n\t" \
  "v_exp_f32_e32 v63, v63\n\t" \
  "s_waitcnt lgkmcnt(0)\n\t" \
  "v_mfma_f32_32x32x16_bf16 v[32:47], v[188:191], v[176:179], 0\n\t" \
  "v_exp_f32_e32 v64, v64\n\t" \
  "v_exp_f32_e32 v65, v65\n\t" \
  "v_exp_f32_e32 v66, v66\n\t" \
  "ds_read_b128 v[192:195], v246 offset:5120\n\t" \
  "ds_read_b64 v[198:199], v247 offset:2560\n\t" \
  "s_add_u32 m0, %[dlds], 118784\n\t" \
  "s_add_u32 %[t0], %[dsrc], 20480\n\t" \
  "buffer_load_dwordx4 v245, %[srd], %[t0] offen sc1 lds\n\t" \
  "buffer_load_dwordx4 v[224:227], v245, %[srd], %[fxo] offen sc1\n\t" \
  "s_add_u32 %[fxo], %[fxo], 0x400\n\t" \
  "v_smfmac_f32_32x32x32_bf16 v[112:127], v[208:211], v[56:63], v244\n\t" \
  "v_exp_f32_e32 v67, v67\n\t" \
  "v_exp_f32_e32 v68, v68\n\t" \
  "ds_read_b128 v[208:211], v248 offset:6048\n\t" \
  "v_mfma_f32_32x32x8_bf16 v[32:47], v[196:197], v[184:185], v[32:47]\n\t" \
  "v_exp_f32_e32 v69, v69\n\t" \
  "v_exp_f32_e32 v70, v70\n\t" \
  "v_exp_f32_e32 v71, v71\n\t" \
  "v_exp_f32_e32 v72, v72\n\t" \
  "s_waitcnt vmcnt(4)\n\t" \
  "v_mfma_f32_32x32x16_bf16 v[128:143], v[232:235], v[228:231], v[128:143]\n\t" \
  "v_exp_f32_e32 v73, v73\n\t" \
  "v_exp_f32_e32 v74, v74\n\t" \
  "v_exp_f32_e32 v75, v75\n\t" \
  "buffer_load_dwordx4 v[232:235], v245, %[srd], %[fwo] offen sc1\n\t" \
  "v_smfmac_f32_32x32x32_bf16 v[96:111], v[204:207], v[64:71], v244\n\t" \
  "v_exp_f32_e32 v76, v76\n\t" \
  "v_exp_f32_e32 v77, v77\n\t" \
  "ds_read_b128 v[200:203], v248 offset:5376\n\t" \
  "v_mfma_f32_32x32x16_bf16 v[48:63], v[188:191], v[180:183], 0\n\t" \
  "v_exp_f32_e32 v78, v78\n\t" \
  "v_exp_f32_e32 v79, v79\n\t" \
  "v_exp_f32_e32 v80, v80\n\t" \
  "s_waitcnt vmcnt(4)\n\t" \
  "v_mfma_f32_32x32x16_bf16 v[144:159], v[236:239], v[228:231], v[144:159]\n\t" \
  "v_exp_f32_e32 v81, v81\n\t" \
  "v_exp_f32_e32 v82, v82\n\t" \
  "v_exp_f32_e32 v83, v83\n\t" \
  "buffer_load_dwordx4 v[236:239], v245, %[srd], %[fwo] offen offset:1024 sc1\n\t" \
  "v_smfmac_f32_32x32x32_bf16 v[96:111], v[212:215], v[72:79], v244\n\t" \
  "v_exp_f32_e32 v84, v84\n\t" \
  "v_exp_f32_e32 v85, v85\n\t" \
  "v_mfma_f32_32x32x8_bf16 v[48:63], v[196:197], v[186:187], v[48:63]\n\t" \
  "v_exp_f32_e32 v86, v86\n\t" \
  "v_exp_f32_e32 v87, v87\n\t" \
  "v_exp_f32_e32 v88, v88\n\t" \
  "v_exp_f32_e32 v89, v89\n\t" \
  "s_waitcnt vmcnt(4)\n\t" \
  "v_mfma_f32_32x32x16_bf16 v[160:175], v[240:243], v[228:231], v[160:175]\n\t" \
  "v_exp_f32_e32 v90, v90\n\t" \
  "v_exp_f32_e32 v91, v91\n\t" \
  "v_exp_f32_e32 v92, v92\n\t" \
  "buffer_load_dwordx4 v[240:243], v252, %[srd], %[fwo] offen offset:2048 sc1\n\t" \
  "s_add_u32 %[fwo], %[fwo], 0xc00\n\t" \
  "v_smfmac_f32_32x32x32_bf16 v[112:127], v[204:207], v[80:87], v244\n\t" \
  "v_exp_f32_e32 v93, v93\n\t" \
  "v_exp_f32_e32 v94, v94\n\t" \
  "v_exp_f32_e32 v95, v95\n\t" \
  "s_waitcnt lgkmcnt(0)\n\t" \
  "v_mfma_f32_32x32x16_bf16 v[64:79], v[192:195], v[176:179], 0\n\t" \
  "v_exp_f32_e32 v32, v32\n\t" \
  "v_exp_f32_e32 v33, v33\n\t" \
  "v_exp_f32_e32 v34, v34\n\t" \
  "ds_read_b128 v[188:191], v246 offset:6144\n\t" \
  "ds_read_b64 v[196:197], v247 offset:3072\n\t" \
  "buffer_load_dwordx4 v[228:231], v245, %[srd], %[fxo] offen sc1\n\t" \
  "s_add_u32 %[fxo], %[fxo], 0x400\n\t" \
  "v_smfmac_f32_32x32x32_bf16 v[112:127], v[212:215], v[88:95], v244\n\t" \
  "v_exp_f32_e32 v35, v35\n\t" \
  "v_exp_f32_e32 v36, v36\n\t" \
  "ds_read_b128 v[212:215], v248 offset:7392\n\t" \
  "v_mfma_f32_32x32x8_bf16 v[64:79], v[198:199], v[184:185], v[64:79]\n\t" \
  "v_exp_f32_e32 v37, v37\n\t" \
  "v_exp_f32_e32 v38, v38\n\t" \
  "v_exp_f32_e32 v39, v39\n\t" \
  "v_exp_f32_e32 v40, v40\n\t" \
  "s_waitcnt vmcnt(3)\n\t" \
  "v_mfma_f32_32x32x16_bf16 v[128:143], v[232:235], v[216:219], v[128:143]\n\t" \
  "v_exp_f32_e32 v41, v41\n\t" \
  "v_exp_f32_e32 v42, v42\n\t" \
  "v_exp_f32_e32 v43, v43\n\t" \
  "buffer_load_dwordx4 v[232:235], v245, %[srd], %[fwo] offen sc1\n\t" \
  "v_smfmac_f32_32x32x32_bf16 v[96:111], v[200:203], v[32:39], v244\n\t" \
  "v_exp_f32_e32 v44, v44\n\t" \
  "v_exp_f32_e32 v45, v45\n\t" \
  "ds_read_b128 v[204:207], v248 offset:6720\n\t" \
  "v_mfma_f32_32x32x16_bf16 v[80:95], v[192:195], v[180:183], 0\n\t" \
  "v_exp_f32_e32 v46, v46\n\t" \
  "v_exp_f32_e32 v47, v47\n\t" \
  "v_exp_f32_e32 v48, v48\n\t" \
  "s_waitcnt vmcnt(3)\n\t" \
  "v_mfma_f32_32x32x16_bf16 v[144:159], v[236:239], v[216:219], v[144:159]\n\t" \
  "v_exp_f32_e32 v49, v49\n\t" \
  "v_exp_f32_e32 v50, v50\n\t" \
  "v_exp_f32_e32 v51, v51\n\t" \
  "buffer_load_dwordx4 v[236:239], v245, %[srd], %[fwo] offen offset:1024 sc1\n\t" \
  "v_smfmac_f32_32x32x32_bf16 v[96:111], v[208:211], v[40:47], v244\n\t" \
  "v_exp_f32_e32 v52, v52\n\t" \
  "v_exp_f32_e32 v53, v53\n\t" \
  "v_mfma_f32_32x32x8_bf16 v[80:95], v[198:199], v[186:187], v[80:95]\n\t" \
  "v_exp_f32_e32 v54, v54\n\t" \
  "v_exp_f32_e32 v55, v55\n\t" \
  "v_exp_f32_e32 v56, v56\n\t" \
  "v_exp_f32_e32 v57, v57\n\t" \
  "s_waitcnt vmcnt(3)\n\t" \
  "v_mfma_f32_32x32x16_bf16 v[160:175], v[240:243], v[216:219], v[160:175]\n\t" \
  "v_exp_f32_e32 v58, v58\n\t" \
  "v_exp_f32_e32 v59, v59\n\t" \
  "v_exp_f32_e32 v60, v60\n\t" \
  "buffer_load_dwordx4 v[240:243], v252, %[srd], %[fwo] offen offset:2048 sc1\n\t" \
  "s_add_u32 %[fwo], %[fwo], 0xc00\n\t" \
  "v_smfmac_f32_32x32x32_bf16 v[112:127], v[200:203], v[48:55], v244\n\t" \
  "v_exp_f32_e32 v61, v61\n\t" \
  "v_exp_f32_e32 v62, v62\n\t" \
  "v_exp_f32_e32 v63, v63\n\t" \
  "s_waitcnt lgkmcnt(0)\n\t" \
  "v_mfma_f32_32x32x16_bf16 v[32:47], v[188:191], v[176:179], 0\n\t" \
  "v_exp_f32_e32 v64, v64\n\t" \
  "v_exp_f32_e32 v65, v65\n\t" \
  "v_exp_f32_e32 v66, v66\n\t" \
  "ds_read_b128 v[192:195], v246 offset:7168\n\t" \
  "ds_read_b64 v[198:199], v247 offset:3584\n\t" \
  "buffer_load_dwordx4 v[216:219], v245, %[srd], %[fxo] offen sc1\n\t" \
  "s_add_u32 %[fxo], %[fxo], 0x400\n\t" \
  "v_smfmac_f32_32x32x32_bf16 v[112:127], v[208:211], v[56:63], v244\n\t" \
  "v_exp_f32_e32 v67, v67\n\t" \
  "v_exp_f32_e32 v68, v68\n\t" \
  "ds_read_b128 v[208:211], v248 offset:8736\n\t" \
  "v_mfma_f32_32x32x8_bf16 v[32:47], v[196:197], v[184:185], v[32:47]\n\t" \
  "v_exp_f32_e32 v69, v69\n\t" \
  "v_exp_f32_e32 v70, v70\n\t" \
  "v_exp_f32_e32 v71, v71\n\t" \
  "v_exp_f32_e32 v72, v72\n\t" \
  "s_waitcnt vmcnt(3)\n\t" \
  "v_mfma_f32_32x32x16_bf16 v[128:143], v[232:235], v[220:223], v[128:143]\n\t" \
  "v_exp_f32_e32 v73, v73\n\t" \
  "v_exp_f32_e32 v74, v74\n\t" \
  "v_exp_f32_e32 v75, v75\n\t" \
  "buffer_load_dwordx4 v[232:235], v245, %[srd], %[fwo] offen sc1\n\t" \
  "v_smfmac_f32_32x32x32_bf16 v[96:111], v[204:207], v[64:71], v244\n\t" \
  "v_exp_f32_e32 v76, v76\n\t" \
  "v_exp_f32_e32 v77, v77\n\t" \
  "ds_read_b128 v[200:203], v248 offset:8064\n\t" \
  "v_mfma_f32_32x32x16_bf16 v[48:63], v[188:191], v[180:183], 0\n\t" \
  "v_exp_f32_e32 v78, v78\n\t" \
  "v_exp_f32_e32 v79, v79\n\t" \
  "v_exp_f32_e32 v80, v80\n\t" \
  "s_waitcnt vmcnt(3)\n\t" \
  "v_mfma_f32_32x32x16_bf16 v[144:159], v[236:239], v[220:223], v[144:159]\n\t" \
  "v_exp_f32_e32 v81, v81\n\t" \
  "v_exp_f32_e32 v82, v82\n\t" \
  "v_exp_f32_e32 v83, v83\n\t" \
  "buffer_load_dwordx4 v[236:239], v245, %[srd], %[fwo] offen offset:1024 sc1\n\t" \
  "v_smfmac_f32_32x32x32_bf16 v[96:111], v[212:215], v[72:79], v244\n\t" \
  "v_exp_f32_e32 v84, v84\n\t" \
  "v_exp_f32_e32 v85, v85\n\t" \
  "v_mfma_f32_32x32x8_bf16 v[48:63], v[196:197], v[186:187], v[48:63]\n\t" \
  "v_exp_f32_e32 v86, v86\n\t" \
  "v_exp_f32_e32 v87, v87\n\t" \
  "v_exp_f32_e32 v88, v88\n\t" \
  "v_exp_f32_e32 v89, v89\n\t" \
  "s_waitcnt vmcnt(3)\n\t" \
  "v_mfma_f32_32x32x16_bf16 v[160:175], v[240:243], v[220:223], v[160:175]\n\t" \
  "v_exp_f32_e32 v90, v90\n\t" \
  "v_exp_f32_e32 v91, v91\n\t" \
  "v_exp_f32_e32 v92, v92\n\t" \
  "buffer_load_dwordx4 v[240:243], v252, %[srd], %[fwo] offen offset:2048 sc1\n\t" \
  "s_add_u32 %[fwo], %[fwo], 0xc00\n\t" \
  "v_smfmac_f32_32x32x32_bf16 v[112:127], v[204:207], v[80:87], v244\n\t" \
  "v_exp_f32_e32 v93, v93\n\t" \
  "v_exp_f32_e32 v94, v94\n\t" \
  "v_exp_f32_e32 v95, v95\n\t" \
  "s_waitcnt lgkmcnt(0)\n\t" \
  "s_barrier\n\t" \
  "v_mfma_f32_32x32x16_bf16 v[64:79], v[192:195], v[176:179], 0\n\t" \
  "v_exp_f32_e32 v32, v32\n\t" \
  "v_exp_f32_e32 v33, v33\n\t" \
  "v_exp_f32_e32 v34, v34\n\t" \
  "ds_read_b128 v[188:191], v249 offset:0\n\t" \
  "ds_read_b64 v[196:197], v250 offset:0\n\t" \
  "s_add_u32 m0, %[dlds], 0\n\t" \
  "s_add_u32 %[t0], %[dsrc2], 0\n\t" \
  "buffer_load_dwordx4 v245, %[srd], %[t0] offen sc1 lds\n\t" \
  "buffer_load_dwordx4 v[220:223], v245, %[srd], %[fxo] offen sc1\n\t" \
  "s_add_u32 %[fxo], %[fxo], 0x400\n\t" \
  "v_smfmac_f32_32x32x32_bf16 v[112:127], v[212:215], v[88:95], v244\n\t" \
  "v_exp_f32_e32 v35, v35\n\t" \
  "v_exp_f32_e32 v36, v36\n\t" \
  "ds_read_b128 v[212:215], v248 offset:10080\n\t" \
  "v_mfma_f32_32x32x8_bf16 v[64:79], v[198:199], v[184:185], v[64:79]\n\t" \
  "v_exp_f32_e32 v37, v37\n\t" \
  "v_exp_f32_e32 v38, v38\n\t" \
  "v_exp_f32_e32 v39, v39\n\t" \
  "v_exp_f32_e32 v40, v40\n\t" \
  "s_waitcnt vmcnt(4)\n\t" \
  "v_mfma_f32_32x32x16_bf16 v[128:143], v[232:235], v[224:227], v[128:143]\n\t" \
  "v_exp_f32_e32 v41, v41\n\t" \
  "v_exp_f32_e32 v42, v42\n\t" \
  "v_exp_f32_e32 v43, v43\n\t" \
  "buffer_load_dwordx4 v[232:235], v245, %[srd], %[fwo] offen sc1\n\t" \
  "v_smfmac_f32_32x32x32_bf16 v[96:111], v[200:203], v[32:39], v244\n\t" \
  "v_exp_f32_e32 v44, v44\n\t" \
  "v_exp_f32_e32 v45, v45\n\t" \
  "ds_read_b128 v[204:207], v248 offset:9408\n\t" \
  "v_mfma_f32_32x32x16_bf16 v[80:95], v[192:195], v[180:183], 0\n\t" \
  "v_exp_f32_e32 v46, v46\n\t" \
  "v_exp_f32_e32 v47, v47\n\t" \
  "v_exp_f32_e32 v48, v48\n\t" \
  "s_waitcnt vmcnt(4)\n\t" \
  "v_mfma_f32_32x32x16_bf16 v[144:159], v[236:239], v[224:227], v[144:159]\n\t" \
  "v_exp_f32_e32 v49, v49\n\t" \
  "v_exp_f32_e32 v50, v50\n\t" \
  "v_exp_f32_e32 v51, v51\n\t" \
  "buffer_load_dwordx4 v[236:239], v245, %[srd], %[fwo] offen offset:1024 sc1\n\t" \
  "v_smfmac_f32_32x32x32_bf16 v[96:111], v[208:211], v[40:47], v244\n\t" \
  "v_exp_f32_e32 v52, v52\n\t" \
  "v_exp_f32_e32 v53, v53\n\t" \
  "v_mfma_f32_32x32x8_bf16 v[80:95], v[198:199], v[186:187], v[80:95]\n\t" \
  "v_exp_f32_e32 v54, v54\n\t" \
  "v_exp_f32_e32 v55, v55\n\t" \
  "v_exp_f32_e32 v56, v56\n\t" \
  "v_exp_f32_e32 v57, v57\n\t" \
  "s_waitcnt vmcnt(4)\n\t" \
  "v_mfma_f32_32x32x16_bf16 v[160:175], v[240:243], v[224:227], v[160:175]\n\t" \
  "v_exp_f32_e32 v58, v58\n\t" \
  "v_exp_f32_e32 v59, v59\n\t" \
  "v_exp_f32_e32 v60, v60\n\t" \
  "buffer_load_dwordx4 v[240:243], v252, %[srd], %[fwo] offen offset:2048 sc1\n\t" \
  "s_add_u32 %[fwo], %[fwo], 0xc00\n\t" \
  "v_smfmac_f32_32x32x32_bf16 v[112:127], v[200:203], v[48:55], v244\n\t" \
  "v_exp_f32_e32 v61, v61\n\t" \
  "v_exp_f32_e32 v62, v62\n\t" \
  "v_exp_f32_e32 v63, v63\n\t" \
  "s_waitcnt lgkmcnt(0)\n\t" \
  "v_mfma_f32_32x32x16_bf16 v[32:47], v[188:191], v[176:179], 0\n\t" \
  "v_exp_f32_e32 v64, v64\n\t" \
  "v_exp_f32_e32 v65, v65\n\t" \
  "v_exp_f32_e32 v66, v66\n\t" \
  "ds_read_b128 v[192:195], v249 offset:1024\n\t" \
  "ds_read_b64 v[198:199], v250 offset:512\n\t" \
  "s_add_u32 m0, %[dlds], 4096\n\t" \
  "s_add_u32 %[t0], %[dsrc2], 4096\n\t" \
  "buffer_load_dwordx4 v245, %[srd], %[t0] offen sc1 lds\n\t" \
  "buffer_load_dwordx4 v[224:227], v245, %[srd], %[fxo] offen sc1\n\t" \
  "s_add_u32 %[fxo], %[fxo], 0x400\n\t" \
  "v_smfmac_f32_32x32x32_bf16 v[112:127], v[208:211], v[56:63], v244\n\t" \
  "v_exp_f32_e32 v67, v67\n\t" \
  "v_exp_f32_e32 v68, v68\n\t" \
  "ds_read_b128 v[208:211], v251 offset:672\n\t" \
  "v_mfma_f32_32x32x8_bf16 v[32:47], v[196:197], v[184:185], v[32:47]\n\t" \
  "v_exp_f32_e32 v69, v69\n\t" \
  "v_exp_f32_e32 v70, v70\n\t" \
  "v_exp_f32_e32 v71, v71\n\t" \
  "v_exp_f32_e32 v72, v72\n\t" \
  "s_waitcnt vmcnt(4)\n\t" \
  "v_mfma_f32_32x32x16_bf16 v[128:143], v[232:235], v[228:231], v[128:143]\n\t" \
  "v_exp_f32_e32 v73, v73\n\t" \
  "v_exp_f32_e32 v74, v74\n\t" \
  "v_exp_f32_e32 v75, v75\n\t" \
  "buffer_load_dwordx4 v[232:235], v245, %[srd], %[fwo] offen sc1\n\t" \
  "v_smfmac_f32_32x32x32_bf16 v[96:111], v[204:207], v[64:71], v244\n\t" \
  "v_exp_f32_e32 v76, v76\n\t" \
  "v_exp_f32_e32 v77, v77\n\t" \
  "ds_read_b128 v[200:203], v251 offset:0\n\t" \
  "v_mfma_f32_32x32x16_bf16 v[48:63], v[188:191], v[180:183], 0\n\t" \
  "v_exp_f32_e32 v78, v78\n\t" \
  "v_exp_f32_e32 v79, v79\n\t" \
  "v_exp_f32_e32 v80, v80\n\t" \
  "s_waitcnt vmcnt(4)\n\t" \
  "v_mfma_f32_32x32x16_bf16 v[144:159], v[236:239], v[228:231], v[144:159]\n\t" \
  "v_exp_f32_e32 v81, v81\n\t" \
  "v_exp_f32_e32 v82, v82\n\t" \
  "v_exp_f32_e32 v83, v83\n\t" \
  "buffer_load_dwordx4 v[236:239], v245, %[srd], %[fwo] offen offset:1024 sc1\n\t" \
  "v_smfmac_f32_32x32x32_bf16 v[96:111], v[212:215], v[72:79], v244\n\t" \
  "v_exp_f32_e32 v84, v84\n\t" \
  "v_exp_f32_e32 v85, v85\n\t" \
  "v_mfma_f32_32x32x8_bf16 v[48:63], v[196:197], v[186:187], v[48:63]\n\t" \
  "v_exp_f32_e32 v86, v86\n\t" \
  "v_exp_f32_e32 v87, v87\n\t" \
  "v_exp_f32_e32 v88, v88\n\t" \
  "v_exp_f32_e32 v89, v89\n\t" \
  "s_waitcnt vmcnt(4)\n\t" \
  "v_mfma_f32_32x32x16_bf16 v[160:175], v[240:243], v[228:231], v[160:175]\n\t" \
  "v_exp_f32_e32 v90, v90\n\t" \
  "v_exp_f32_e32 v91, v91\n\t" \
  "v_exp_f32_e32 v92, v92\n\t" \
  "buffer_load_dwordx4 v[240:243], v252, %[srd], %[fwo] offen offset:2048 sc1\n\t" \
  "s_add_u32 %[fwo], %[fwo], 0xc00\n\t" \
  "v_smfmac_f32_32x32x32_bf16 v[112:127], v[204:207], v[80:87], v244\n\t" \
  "v_exp_f32_e32 v93, v93\n\t" \
  "v_exp_f32_e32 v94, v94\n\t" \
  "v_exp_f32_e32 v95, v95\n\t" \
  "v_mov_b32_e32 v246, v249\n\t" \
  "v_mov_b32_e32 v247, v250\n\t" \
  "v_mov_b32_e32 v248, v251\n\t" \
  "v_add_u32_e32 v249, 49152, v249\n\t" \
  "v_add_u32_e32 v250, 49152, v250\n\t" \
  "v_add_u32_e32 v251, 49152, v251\n\t" \
  "s_waitcnt lgkmcnt(0)\n\t" \
  "v_mfma_f32_32x32x16_bf16 v[64:79], v[192:195], v[176:179], 0\n\t" \
  "v_exp_f32_e32 v32, v32\n\t" \
  "v_exp_f32_e32 v33, v33\n\t" \
  "v_exp_f32_e32 v34, v34\n\t" \
  "ds_read_b128 v[188:191], v246 offset:2048\n\t" \
  "ds_read_b64 v[196:197], v247 offset:1024\n\t" \
  "s_add_u32 m0, %[dlds], 8192\n\t" \
  "s_add_u32 %[t0], %[dsrc2], 8192\n\t" \
  "buffer_load_dwordx4 v245, %[srd], %[t0] offen sc1 lds\n\t" \
  "buffer_load_dwordx4 v[228:231], v245, %[srd], %[fxo] offen sc1\n\t" \
  "s_add_u32 %[fxo], %[fxo], 0x400\n\t" \
  "v_smfmac_f32_32x32x32_bf16 v[112:127], v[212:215], v[88:95], v244\n\t" \
  "v_exp_f32_e32 v35, v35\n\t" \
  "v_exp_f32_e32 v36, v36\n\t" \
  "ds_read_b128 v[212:215], v248 offset:2016\n\t" \
  "v_mfma_f32_32x32x8_bf16 v[64:79], v[198:199], v[184:185], v[64:79]\n\t" \
  "v_exp_f32_e32 v37, v37\n\t" \
  "v_exp_f32_e32 v38, v38\n\t" \
  "v_exp_f32_e32 v39, v39\n\t" \
  "v_exp_f32_e32 v40, v40\n\t" \
  "s_waitcnt vmcnt(4)\n\t" \
  "v_mfma_f32_32x32x16_bf16 v[128:143], v[232:235], v[216:219], v[128:143]\n\t" \
  "v_exp_f32_e32 v41, v41\n\t" \
  "v_exp_f32_e32 v42, v42\n\t" \
  "v_exp_f32_e32 v43, v43\n\t" \
  "buffer_load_dwordx4 v[232:235], v245, %[srd], %[fwo] offen sc1\n\t" \
  "v_smfmac_f32_32x32x32_bf16 v[96:111], v[200:203], v[32:39], v244\n\t" \
  "v_exp_f32_e32 v44, v44\n\t" \
  "v_exp_f32_e32 v45, v45\n\t" \
  "ds_read_b128 v[204:207], v248 offset:1344\n\t" \
  "v_mfma_f32_32x32x16_bf16 v[80:95], v[192:195], v[180:183], 0\n\t" \
  "v_exp_f32_e32 v46, v46\n\t" \
  "v_exp_f32_e32 v47, v47\n\t" \
  "v_exp_f32_e32 v48, v48\n\t" \
  "s_waitcnt vmcnt(4)\n\t" \
  "v_mfma_f32_32x32x16_bf16 v[144:159], v[236:239], v[216:219], v[144:159]\n\t" \
  "v_exp_f32_e32 v49, v49\n\t" \
  "v_exp_f32_e32 v50, v50\n\t" \
  "v_exp_f32_e32 v51, v51\n\t" \
  "buffer_load_dwordx4 v[236:239], v245, %[srd], %[fwo] offen offset:1024 sc1\n\t" \
  "v_smfmac_f32_32x32x32_bf16 v[96:111], v[208:211], v[40:47], v244\n\t" \
  "v_exp_f32_e32 v52, v52\n\t" \
  "v_exp_f32_e32 v53, v53\n\t" \
  "v_mfma_f32_32x32x8_bf16 v[80:95], v[198:199], v[186:187], v[80:95]\n\t" \
  "v_exp_f32_e32 v54, v54\n\t" \
  "v_exp_f32_e32 v55, v55\n\t" \
  "v_exp_f32_e32 v56, v56\n\t" \
  "v_exp_f32_e32 v57, v57\n\t" \
  "s_waitcnt vmcnt(4)\n\t" \
  "v_mfma_f32_32x32x16_bf16 v[160:175], v[240:243], v[216:219], v[160:175]\n\t" \
  "v_exp_f32_e32 v58, v58\n\t" \
  "v_exp_f32_e32 v59, v59\n\t" \
  "v_exp_f32_e32 v60, v60\n\t" \
  "buffer_load_dwordx4 v[240:243], v252, %[srd], %[fwo] offen offset:2048 sc1\n\t" \
  "s_add_u32 %[fwo], %[fwo], 0xc00\n\t" \
  "v_smfmac_f32_32x32x32_bf16 v[112:127], v[200:203], v[48:55], v244\n\t" \
  "v_exp_f32_e32 v61, v61\n\t" \
  "v_exp_f32_e32 v62, v62\n\t" \
  "v_exp_f32_e32 v63, v63\n\t" \
  "s_waitcnt lgkmcnt(0)\n\t" \
  "v_mfma_f32_32x32x16_bf16 v[32:47], v[188:191], v[176:179], 0\n\t" \
  "v_exp_f32_e32 v64, v64\n\t" \
  "v_exp_f32_e32 v65, v65\n\t" \
  "v_exp_f32_e32 v66, v66\n\t" \
  "ds_read_b128 v[192:195], v246 offset:3072\n\t" \
  "ds_read_b64 v[198:199], v247 offset:1536\n\t" \
  "s_add_u32 m0, %[dlds], 12288\n\t" \
  "s_add_u32 %[t0], %[dsrc2], 12288\n\t" \
  "buffer_load_dwordx4 v245, %[srd], %[t0] offen sc1 lds\n\t" \
  "buffer_load_dwordx4 v[216:219], v245, %[srd], %[fxo] offen sc1\n\t" \
  "s_add_u32 %[fxo], %[fxo], 0x400\n\t" \
  "v_smfmac_f32_32x32x32_bf16 v[112:127], v[208:211], v[56:63], v244\n\t" \
  "v_exp_f32_e32 v67, v67\n\t" \
  "v_exp_f32_e32 v68, v68\n\t" \
  "ds_read_b128 v[208:211], v248 offset:3360\n\t" \
  "v_mfma_f32_32x32x8_bf16 v[32:47], v[196:197], v[184:185], v[32:47]\n\t" \
  "v_exp_f32_e32 v69, v69\n\t" \
  "v_exp_f32_e32 v70, v70\n\t" \
  "v_exp_f32_e32 v71, v71\n\t" \
  "v_exp_f32_e32 v72, v72\n\t" \
  "s_waitcnt vmcnt(4)\n\t" \
  "v_mfma_f32_32x32x16_bf16 v[128:143], v[232:235], v[220:223], v[128:143]\n\t" \
  "v_exp_f32_e32 v73, v73\n\t" \
  "v_exp_f32_e32 v74, v74\n\t" \
  "v_exp_f32_e32 v75, v75\n\t" \
  "buffer_load_dwordx4 v[232:235], v245, %[srd], %[fwo] offen sc1\n\t" \
  "v_smfmac_f32_32x32x32_bf16 v[96:111], v[204:207], v[64:71], v244\n\t" \
  "v_exp_f32_e32 v76, v76\n\t" \
  "v_exp_f32_e32 v77, v77\n\t" \
  "ds_read_b128 v[200:203], v248 offset:2688\n\t" \
  "v_mfma_f32_32x32x16_bf16 v[48:63], v[188:191], v[180:183], 0\n\t" \
  "v_exp_f32_e32 v78, v78\n\t" \
  "v_exp_f32_e32 v79, v79\n\t" \
  "v_exp_f32_e32 v80, v80\n\t" \
  "s_waitcnt vmcnt(4)\n\t" \
  "v_mfma_f32_32x32x16_bf16 v[144:159], v[236:239], v[220:223], v[144:159]\n\t" \
  "v_exp_f32_e32 v81, v81\n\t" \
  "v_exp_f32_e32 v82, v82\n\t" \
  "v_exp_f32_e32 v83, v83\n\t" \
  "buffer_load_dwordx4 v[236:239], v245, %[srd], %[fwo] offen offset:1024 sc1\n\t" \
  "v_smfmac_f32_32x32x32_bf16 v[96:111], v[212:215], v[72:79], v244\n\t" \
  "v_exp_f32_e32 v84, v84\n\t" \
  "v_exp_f32_e32 v85, v85\n\t" \
  "v_mfma_f32_32x32x8_bf16 v[48:63], v[196:197], v[186:187], v[48:63]\n\t" \
  "v_exp_f32_e32 v86, v86\n\t" \
  "v_exp_f32_e32 v87, v87\n\t" \
  "v_exp_f32_e32 v88, v88\n\t" \
  "v_exp_f32_e32 v89, v89\n\t" \
  "s_waitcnt vmcnt(4)\n\t" \
  "v_mfma_f32_32x32x16_bf16 v[160:175], v[240:243], v[220:223], v[160:175]\n\t" \
  "v_exp_f32_e32 v90, v90\n\t" \
  "v_exp_f32_e32 v91, v91\n\t" \
  "v_exp_f32_e32 v92, v92\n\t" \
  "buffer_load_dwordx4 v[240:243], v252, %[srd], %[fwo] offen offset:2048 sc1\n\t" \
  "s_add_u32 %[fwo], %[fwo], 0xc00\n\t" \
  "v_smfmac_f32_32x32x32_bf16 v[112:127], v[204:207], v[80:87], v244\n\t" \
  "v_exp_f32_e32 v93, v93\n\t" \
  "v_exp_f32_e32 v94, v94\n\t" \
  "v_exp_f32_e32 v95, v95\n\t" \
  "s_waitcnt lgkmcnt(0)\n\t" \
  "v_mfma_f32_32x32x16_bf16 v[64:79], v[192:195], v[176:179], 0\n\t" \
  "v_exp_f32_e32 v32, v32\n\t" \
  "v_exp_f32_e32 v33, v33\n\t" \
  "v_exp_f32_e32 v34, v34\n\t" \
  "ds_read_b128 v[188:191], v246 offset:4096\n\t" \
  "ds_read_b64 v[196:197], v247 offset:2048\n\t" \
  "s_add_u32 m0, %[dlds], 16384\n\t" \
  "s_add_u32 %[t0], %[dsrc2], 16384\n\t" \
  "buffer_load_dwordx4 v245, %[srd], %[t0] offen sc1 lds\n\t" \
  "v_smfmac_f32_32x32x32_bf16 v[112:127], v[212:215], v[88:95], v244\n\t" \
  "v_exp_f32_e32 v35, v35\n\t" \
  "v_exp_f32_e32 v36, v36\n\t" \
  "ds_read_b128 v[212:215], v248 offset:4704\n\t" \
  "v_mfma_f32_32x32x8_bf16 v[64:79], v[198:199], v[184:185], v[64:79]\n\t" \
  "v_exp_f32_e32 v37, v37\n\t" \
  "v_exp_f32_e32 v38, v38\n\t" \
  "v_exp_f32_e32 v39, v39\n\t" \
  "v_exp_f32_e32 v40, v40\n\t" \
  "s_waitcnt vmcnt(3)\n\t" \
  "v_mfma_f32_32x32x16_bf16 v[128:143], v[232:235], v[224:227], v[128:143]\n\t" \
  "v_exp_f32_e32 v41, v41\n\t" \
  "v_exp_f32_e32 v42, v42\n\t" \
  "v_exp_f32_e32 v43, v43\n\t" \
  "buffer_load_dwordx4 v[232:235], v245, %[srd], %[fwo] offen sc1\n\t" \
  "v_smfmac_f32_32x32x32_bf16 v[96:111], v[200:203], v[32:39], v244\n\t" \
  "v_exp_f32_e32 v44, v44\n\t" \
  "v_exp_f32_e32 v45, v45\n\t" \
  "ds_read_b128 v[204:207], v248 offset:4032\n\t" \
  "v_mfma_f32_32x32x16_bf16 v[80:95], v[192:195], v[180:183], 0\n\t" \
  "v_exp_f32_e32 v46, v46\n\t" \
  "v_exp_f32_e32 v47, v47\n\t" \
  "v_exp_f32_e32 v48, v48\n\t" \
  "s_waitcnt vmcnt(3)\n\t" \
  "v_mfma_f32_32x32x16_bf16 v[144:159], v[236:239], v[224:227], v[144:159]\n\t" \
  "v_exp_f32_e32 v49, v49\n\t" \
  "v_exp_f32_e32 v50, v50\n\t" \
  "v_exp_f32_e32 v51, v51\n\t" \
  "buffer_load_dwordx4 v[236:239], v245, %[srd], %[fwo] offen offset:1024 sc1\n\t" \
  "v_smfmac_f32_32x32x32_bf16 v[96:111], v[208:211], v[40:47], v244\n\t" \
  "v_exp_f32_e32 v52, v52\n\t" \
  "v_exp_f32_e32 v53, v53\n\t" \
  "v_mfma_f32_32x32x8_bf16 v[80:95], v[198:199], v[186:187], v[80:95]\n\t" \
  "v_exp_f32_e32 v54, v54\n\t" \
  "v_exp_f32_e32 v55, v55\n\t" \
  "v_exp_f32_e32 v56, v56\n\t" \
  "v_exp_f32_e32 v57, v57\n\t" \
  "s_waitcnt vmcnt(3)\n\t" \
  "v_mfma_f32_32x32x16_bf16 v[160:175], v[240:243], v[224:227], v[160:175]\n\t" \
  "v_exp_f32_e32 v58, v58\n\t" \
  "v_exp_f32_e32 v59, v59\n\t" \
  "v_exp_f32_e32 v60, v60\n\t" \
  "buffer_load_dwordx4 v[240:243], v252, %[srd], %[fwo] offen offset:2048 sc1\n\t" \
  "s_add_u32 %[fwo], %[fwo], 0xc00\n\t" \
  "v_smfmac_f32_32x32x32_bf16 v[112:127], v[200:203], v[48:55], v244\n\t" \
  "v_exp_f32_e32 v61, v61\n\t" \
  "v_exp_f32_e32 v62, v62\n\t" \
  "v_exp_f32_e32 v63, v63\n\t" \
  "s_waitcnt lgkmcnt(0)\n\t" \
  "v_mfma_f32_32x32x16_bf16 v[32:47], v[188:191], v[176:179], 0\n\t" \
  "v_exp_f32_e32 v64, v64\n\t" \
  "v_exp_f32_e32 v65, v65\n\t" \
  "v_exp_f32_e32 v66, v66\n\t" \
  "ds_read_b128 v[192:195], v246 offset:5120\n\t" \
  "ds_read_b64 v[198:199], v247 offset:2560\n\t" \
  "s_add_u32 m0, %[dlds], 20480\n\t" \
  "s_add_u32 %[t0], %[dsrc2], 20480\n\t" \
  "buffer_load_dwordx4 v245, %[srd], %[t0] offen sc1 lds\n\t" \
  "v_smfmac_f32_32x32x32_bf16 v[112:127], v[208:211], v[56:63], v244\n\t" \
  "v_exp_f32_e32 v67, v67\n\t" \
  "v_exp_f32_e32 v68, v68\n\t" \
  "ds_read_b128 v[208:211], v248 offset:6048\n\t" \
  "v_mfma_f32_32x32x8_bf16 v[32:47], v[196:197], v[184:185], v[32:47]\n\t" \
  "v_exp_f32_e32 v69, v69\n\t" \
  "v_exp_f32_e32 v70, v70\n\t" \
  "v_exp_f32_e32 v71, v71\n\t" \
  "v_exp_f32_e32 v72, v72\n\t" \
  "s_waitcnt vmcnt(3)\n\t" \
  "v_mfma_f32_32x32x16_bf16 v[128:143], v[232:235], v[228:231], v[128:143]\n\t" \
  "v_exp_f32_e32 v73, v73\n\t" \
  "v_exp_f32_e32 v74, v74\n\t" \
  "v_exp_f32_e32 v75, v75\n\t" \
  "buffer_load_dwordx4 v[232:235], v245, %[srd], %[fwo] offen sc1\n\t" \
  "v_smfmac_f32_32x32x32_bf16 v[96:111], v[204:207], v[64:71], v244\n\t" \
  "v_exp_f32_e32 v76, v76\n\t" \
  "v_exp_f32_e32 v77, v77\n\t" \
  "ds_read_b128 v[200:203], v248 offset:5376\n\t" \
  "v_mfma_f32_32x32x16_bf16 v[48:63], v[188:191], v[180:183], 0\n\t" \
  "v_exp_f32_e32 v78, v78\n\t" \
  "v_exp_f32_e32 v79, v79\n\t" \
  "v_exp_f32_e32 v80, v80\n\t" \
  "s_waitcnt vmcnt(3)\n\t" \
  "v_mfma_f32_32x32x16_bf16 v[144:159], v[236:239], v[228:231], v[144:159]\n\t" \
  "v_exp_f32_e32 v81, v81\n\t" \
  "v_exp_f32_e32 v82, v82\n\t" \
  "v_exp_f32_e32 v83, v83\n\t" \
  "buffer_load_dwordx4 v[236:239], v245, %[srd], %[fwo] offen offset:1024 sc1\n\t" \
  "v_smfmac_f32_32x32x32_bf16 v[96:111], v[212:215], v[72:79], v244\n\t" \
  "v_exp_f32_e32 v84, v84\n\t" \
  "v_exp_f32_e32 v85, v85\n\t" \
  "v_mfma_f32_32x32x8_bf16 v[48:63], v[196:197], v[186:187], v[48:63]\n\t" \
  "v_exp_f32_e32 v86, v86\n\t" \
  "v_exp_f32_e32 v87, v87\n\t" \
  "v_exp_f32_e32 v88, v88\n\t" \
  "v_exp_f32_e32 v89, v89\n\t" \
  "s_waitcnt vmcnt(3)\n\t" \
  "v_mfma_f32_32x32x16_bf16 v[160:175], v[240:243], v[228:231], v[160:175]\n\t" \
  "v_exp_f32_e32 v90, v90\n\t" \
  "v_exp_f32_e32 v91, v91\n\t" \
  "v_exp_f32_e32 v92, v92\n\t" \
  "buffer_load_dwordx4 v[240:243], v252, %[srd], %[fwo] offen offset:2048 sc1\n\t" \
  "s_add_u32 %[fwo], %[fwo], 0xc00\n\t" \
  "v_smfmac_f32_32x32x32_bf16 v[112:127], v[204:207], v[80:87], v244\n\t" \
  "v_exp_f32_e32 v93, v93\n\t" \
  "v_exp_f32_e32 v94, v94\n\t" \
  "v_exp_f32_e32 v95, v95\n\t" \
  "s_waitcnt lgkmcnt(0)\n\t" \
  "v_mfma_f32_32x32x16_bf16 v[64:79], v[192:195], v[176:179], 0\n\t" \
  "v_exp_f32_e32 v32, v32\n\t" \
  "v_exp_f32_e32 v33, v33\n\t" \
  "v_exp_f32_e32 v34, v34\n\t" \
  "ds_read_b128 v[188:191], v246 offset:6144\n\t" \
  "ds_read_b64 v[196:197], v247 offset:3072\n\t" \
  "v_smfmac_f32_32x32x32_bf16 v[112:127], v[212:215], v[88:95], v244\n\t" \
  "v_exp_f32_e32 v35, v35\n\t" \
  "v_exp_f32_e32 v36, v36\n\t" \
  "ds_read_b128 v[212:215], v248 offset:7392\n\t" \
  "v_mfma_f32_32x32x8_bf16 v[64:79], v[198:199], v[184:185], v[64:79]\n\t" \
  "v_exp_f32_e32 v37, v37\n\t" \
  "v_exp_f32_e32 v38, v38\n\t" \
  "v_exp_f32_e32 v39, v39\n\t" \
  "v_exp_f32_e32 v40, v40\n\t" \
  "s_waitcnt vmcnt(2)\n\t" \
  "v_mfma_f32_32x32x16_bf16 v[128:143], v[232:235], v[216:219], v[128:143]\n\t" \
  "v_exp_f32_e32 v41, v41\n\t" \
  "v_exp_f32_e32 v42, v42\n\t" \
  "v_exp_f32_e32 v43, v43\n\t" \
  "v_smfmac_f32_32x32x32_bf16 v[96:111], v[200:203], v[32:39], v244\n\t" \
  "v_exp_f32_e32 v44, v44\n\t" \
  "v_exp_f32_e32 v45, v45\n\t" \
  "ds_read_b128 v[204:207], v248 offset:6720\n\t" \
  "v_mfma_f32_32x32x16_bf16 v[80:95], v[192:195], v[180:183], 0\n\t" \
  "v_exp_f32_e32 v46, v46\n\t" \
  "v_exp_f32_e32 v47, v47\n\t" \
  "v_exp_f32_e32 v48, v48\n\t" \
  "s_waitcnt vmcnt(1)\n\t" \
  "v_mfma_f32_32x32x16_bf16 v[144:159], v[236:239], v[216:219], v[144:159]\n\t" \
  "v_exp_f32_e32 v49, v49\n\t" \
  "v_exp_f32_e32 v50, v50\n\t" \
  "v_exp_f32_e32 v51, v51\n\t" \
  "v_smfmac_f32_32x32x32_bf16 v[96:111], v[208:211], v[40:47], v244\n\t" \
  "v_exp_f32_e32 v52, v52\n\t" \
  "v_exp_f32_e32 v53, v53\n\t" \
  "v_mfma_f32_32x32x8_bf16 v[80:95], v[198:199], v[186:187], v[80:95]\n\t" \
  "v_exp_f32_e32 v54, v54\n\t" \
  "v_exp_f32_e32 v55, v55\n\t" \
  "v_exp_f32_e32 v56, v56\n\t" \
  "v_exp_f32_e32 v57, v57\n\t" \
  "s_waitcnt vmcnt(0)\n\t" \
  "v_mfma_f32_32x32x16_bf16 v[160:175], v[240:243], v[216:219], v[160:175]\n\t" \
  "v_exp_f32_e32 v58, v58\n\t" \
  "v_exp_f32_e32 v59, v59\n\t" \
  "v_exp_f32_e32 v60, v60\n\t" \
  "v_smfmac_f32_32x32x32_bf16 v[112:127], v[200:203], v[48:55], v244\n\t" \
  "v_exp_f32_e32 v61, v61\n\t" \
  "v_exp_f32_e32 v62, v62\n\t" \
  "v_exp_f32_e32 v63, v63\n\t" \
  "s_waitcnt lgkmcnt(0)\n\t" \
  "buffer_load_dwordx4 v[216:219], v245, %[srd], %[fxo] offen sc1\n\t" \
  "s_add_u32 %[fxo], %[fxo], 0x400\n\t" \
  "buffer_load_dwordx4 v[220:223], v245, %[srd], %[fxo] offen sc1\n\t" \
  "s_add_u32 %[fxo], %[fxo], 0x400\n\t" \
  "buffer_load_dwordx4 v[224:227], v245, %[srd], %[fxo] offen sc1\n\t" \
  "s_add_u32 %[fxo], %[fxo], 0x400\n\t" \
  "buffer_load_dwordx4 v[232:235], v245, %[srd], %[fwo] offen sc1\n\t" \
  "buffer_load_dwordx4 v[236:239], v245, %[srd], %[fwo] offen offset:1024 sc1\n\t" \
  "buffer_load_dwordx4 v[240:243], v252, %[srd], %[fwo] offen offset:2048 sc1\n\t" \
  "s_add_u32 %[fwo], %[fwo], 0xc00\n\t" \
  "v_mfma_f32_32x32x16_bf16 v[32:47], v[188:191], v[176:179], 0\n\t" \
  "v_exp_f32_e32 v64, v64\n\t" \
  "v_exp_f32_e32 v65, v65\n\t" \
  "v_exp_f32_e32 v66, v66\n\t" \
  "v_exp_f32_e32 v67, v67\n\t" \
  "ds_read_b128 v[192:195], v246 offset:7168\n\t" \
  "ds_read_b64 v[198:199], v247 offset:3584\n\t" \
  "v_smfmac_f32_32x32x32_bf16 v[112:127], v[208:211], v[56:63], v244\n\t" \
  "v_exp_f32_e32 v68, v68\n\t" \
  "v_exp_f32_e32 v69, v69\n\t" \
  "v_exp_f32_e32 v70, v70\n\t" \
  "v_exp_f32_e32 v71, v71\n\t" \
  "ds_read_b128 v[208:211], v248 offset:8736\n\t" \
  "v_mfma_f32_32x32x8_bf16 v[32:47], v[196:197], v[184:185], v[32:47]\n\t" \
  "v_exp_f32_e32 v72, v72\n\t" \
  "v_exp_f32_e32 v73, v73\n\t" \
  "v_exp_f32_e32 v74, v74\n\t" \
  "v_exp_f32_e32 v75, v75\n\t" \
  "ds_read_b128 v[200:203], v248 offset:8064\n\t" \
  "v_smfmac_f32_32x32x32_bf16 v[96:111], v[204:207], v[64:71], v244\n\t" \
  "v_exp_f32_e32 v76, v76\n\t" \
  "v_exp_f32_e32 v77, v77\n\t" \
  "v_exp_f32_e32 v78, v78\n\t" \
  "v_exp_f32_e32 v79, v79\n\t" \
  "v_mfma_f32_32x32x16_bf16 v[48:63], v[188:191], v[180:183], 0\n\t" \
  "v_exp_f32_e32 v80, v80\n\t" \
  "v_exp_f32_e32 v81, v81\n\t" \
  "v_exp_f32_e32 v82, v82\n\t" \
  "v_exp_f32_e32 v83, v83\n\t" \
  "v_smfmac_f32_32x32x32_bf16 v[96:111], v[212:215], v[72:79], v244\n\t" \
  "v_exp_f32_e32 v84, v84\n\t" \
  "v_exp_f32_e32 v85, v85\n\t" \
  "v_exp_f32_e32 v86, v86\n\t" \
  "v_exp_f32_e32 v87, v87\n\t" \
  "v_mfma_f32_32x32x8_bf16 v[48:63], v[196:197], v[186:187], v[48:63]\n\t" \
  "v_exp_f32_e32 v88, v88\n\t" \
  "v_exp_f32_e32 v89, v89\n\t" \
  "v_exp_f32_e32 v90, v90\n\t" \
  "v_exp_f32_e32 v91, v91\n\t" \
  "v_smfmac_f32_32x32x32_bf16 v[112:127], v[204:207], v[80:87], v244\n\t" \
  "v_exp_f32_e32 v92, v92\n\t" \
  "v_exp_f32_e32 v93, v93\n\t" \
  "v_exp_f32_e32 v94, v94\n\t" \
  "v_exp_f32_e32 v95, v95\n\t" \
  "s_waitcnt lgkmcnt(0)\n\t" \
  "s_barrier\n\t" \
  "v_mfma_f32_32x32x16_bf16 v[64:79], v[192:195], v[176:179], 0\n\t" \
  "v_exp_f32_e32 v32, v32\n\t" \
  "v_exp_f32_e32 v33, v33\n\t" \
  "v_exp_f32_e32 v34, v34\n\t" \
  "v_exp_f32_e32 v35, v35\n\t" \
  "ds_read_b128 v[188:191], v246 offset:0\n\t" \
  "ds_read_b64 v[196:197], v247 offset:0\n\t" \
  "s_add_u32 m0, %[dlds], 49152\n\t" \
  "s_add_u32 %[t0], %[dsrc2], 49152\n\t" \
  "buffer_load_dwordx4 v245, %[srd], %[t0] offen sc1 lds\n\t" \
  "s_add_u32 m0, %[dlds], 53248\n\t" \
  "s_add_u32 %[t0], %[dsrc2], 53248\n\t" \
  "buffer_load_dwordx4 v245, %[srd], %[t0] offen sc1 lds\n\t" \
  "s_add_u32 m0, %[dlds], 57344\n\t" \
  "s_add_u32 %[t0], %[dsrc2], 57344\n\t" \
  "buffer_load_dwordx4 v245, %[srd], %[t0] offen sc1 lds\n\t" \
  "v_smfmac_f32_32x32x32_bf16 v[112:127], v[212:215], v[88:95], v244\n\t" \
  "v_exp_f32_e32 v36, v36\n\t" \
  "v_exp_f32_e32 v37, v37\n\t" \
  "v_exp_f32_e32 v38, v38\n\t" \
  "v_exp_f32_e32 v39, v39\n\t" \
  "ds_read_b128 v[212:215], v248 offset:10080\n\t" \
  "v_mfma_f32_32x32x8_bf16 v[64:79], v[198:199], v[184:185], v[64:79]\n\t" \
  "v_exp_f32_e32 v40, v40\n\t" \
  "v_exp_f32_e32 v41, v41\n\t" \
  "v_exp_f32_e32 v42, v42\n\t" \
  "v_exp_f32_e32 v43, v43\n\t" \
  "ds_read_b128 v[204:207], v248 offset:9408\n\t" \
  "v_smfmac_f32_32x32x32_bf16 v[96:111], v[200:203], v[32:39], v244\n\t" \
  "v_exp_f32_e32 v44, v44\n\t" \
  "v_exp_f32_e32 v45, v45\n\t" \
  "v_exp_f32_e32 v46, v46\n\t" \
  "v_exp_f32_e32 v47, v47\n\t" \
  "v_mfma_f32_32x32x16_bf16 v[80:95], v[192:195], v[180:183], 0\n\t" \
  "v_exp_f32_e32 v48, v48\n\t" \
  "v_exp_f32_e32 v49, v49\n\t" \
  "v_exp_f32_e32 v50, v50\n\t" \
  "v_exp_f32_e32 v51, v51\n\t" \
  "v_smfmac_f32_32x32x32_bf16 v[96:111], v[208:211], v[40:47], v244\n\t" \
  "v_exp_f32_e32 v52, v52\n\t" \
  "v_exp_f32_e32 v53, v53\n\t" \
  "v_exp_f32_e32 v54, v54\n\t" \
  "v_exp_f32_e32 v55, v55\n\t" \
  "v_mfma_f32_32x32x8_bf16 v[80:95], v[198:199], v[186:187], v[80:95]\n\t" \
  "v_exp_f32_e32 v56, v56\n\t" \
  "v_exp_f32_e32 v57, v57\n\t" \
  "v_exp_f32_e32 v58, v58\n\t" \
  "v_exp_f32_e32 v59, v59\n\t" \
  "v_smfmac_f32_32x32x32_bf16 v[112:127], v[200:203], v[48:55], v244\n\t" \
  "v_exp_f32_e32 v60, v60\n\t" \
  "v_exp_f32_e32 v61, v61\n\t" \
  "v_exp_f32_e32 v62, v62\n\t" \
  "v_exp_f32_e32 v63, v63\n\t" \
  "s_waitcnt lgkmcnt(0)\n\t" \
  "v_mfma_f32_32x32x16_bf16 v[32:47], v[188:191], v[176:179], 0\n\t" \
  "v_exp_f32_e32 v64, v64\n\t" \
  "v_exp_f32_e32 v65, v65\n\t" \
  "v_exp_f32_e32 v66, v66\n\t" \
  "v_exp_f32_e32 v67, v67\n\t" \
  "ds_read_b128 v[192:195], v246 offset:1024\n\t" \
  "ds_read_b64 v[198:199], v247 offset:512\n\t" \
  "s_add_u32 m0, %[dlds], 61440\n\t" \
  "s_add_u32 %[t0], %[dsrc2], 61440\n\t" \
  "buffer_load_dwordx4 v245, %[srd], %[t0] offen sc1 lds\n\t" \
  "s_add_u32 m0, %[dlds], 65536\n\t" \
  "s_add_u32 %[t0], %[dsrc2], 65536\n\t" \
  "buffer_load_dwordx4 v245, %[srd], %[t0] offen sc1 lds\n\t" \
  "s_add_u32 m0, %[dlds], 69632\n\t" \
  "s_add_u32 %[t0], %[dsrc2], 69632\n\t" \
  "buffer_load_dwordx4 v245, %[srd], %[t0] offen sc1 lds\n\t" \
  "v_smfmac_f32_32x32x32_bf16 v[112:127], v[208:211], v[56:63], v244\n\t" \
  "v_exp_f32_e32 v68, v68\n\t" \
  "v_exp_f32_e32 v69, v69\n\t" \
  "v_exp_f32_e32 v70, v70\n\t" \
  "v_exp_f32_e32 v71, v71\n\t" \
  "ds_read_b128 v[208:211], v248 offset:10080\n\t" \
  "v_mfma_f32_32x32x8_bf16 v[32:47], v[196:197], v[184:185], v[32:47]\n\t" \
  "v_exp_f32_e32 v72, v72\n\t" \
  "v_exp_f32_e32 v73, v73\n\t" \
  "v_exp_f32_e32 v74, v74\n\t" \
  "v_exp_f32_e32 v75, v75\n\t" \
  "ds_read_b128 v[200:203], v248 offset:9408\n\t" \
  "v_smfmac_f32_32x32x32_bf16 v[96:111], v[204:207], v[64:71], v244\n\t" \
  "v_exp_f32_e32 v76, v76\n\t" \
  "v_exp_f32_e32 v77, v77\n\t" \
  "v_exp_f32_e32 v78, v78\n\t" \
  "v_exp_f32_e32 v79, v79\n\t" \
  "v_mfma_f32_32x32x16_bf16 v[48:63], v[188:191], v[180:183], 0\n\t" \
  "v_exp_f32_e32 v80, v80\n\t" \
  "v_exp_f32_e32 v81, v81\n\t" \
  "v_exp_f32_e32 v82, v82\n\t" \
  "v_exp_f32_e32 v83, v83\n\t" \
  "v_smfmac_f32_32x32x32_bf16 v[96:111], v[212:215], v[72:79], v244\n\t" \
  "v_exp_f32_e32 v84, v84\n\t" \
  "v_exp_f32_e32 v85, v85\n\t" \
  "v_exp_f32_e32 v86, v86\n\t" \
  "v_exp_f32_e32 v87, v87\n\t" \
  "v_mfma_f32_32x32x8_bf16 v[48:63], v[196:197], v[186:187], v[48:63]\n\t" \
  "v_exp_f32_e32 v88, v88\n\t" \
  "v_exp_f32_e32 v89, v89\n\t" \
  "v_exp_f32_e32 v90, v90\n\t" \
  "v_exp_f32_e32 v91, v91\n\t" \
  "v_smfmac_f32_32x32x32_bf16 v[112:127], v[204:207], v[80:87], v244\n\t" \
  "v_exp_f32_e32 v92, v92\n\t" \
  "v_exp_f32_e32 v93, v93\n\t" \
  "v_exp_f32_e32 v94, v94\n\t" \
  "v_exp_f32_e32 v95, v95\n\t" \
  "s_nop 1\n\t" \
  "v_smfmac_f32_32x32x32_bf16 v[112:127], v[212:215], v[88:95], v244\n\t" \
  "s_waitcnt vmcnt(0) lgkmcnt(0)\n\t" \
  "s_nop 7\n\t" \
  "s_nop 7\n\t" \
  ""
#define ATT_NF_IN 24
#define ATT_CLOBBERS "v32", "v33", "v34", "v35", "v36", "v37", "v38", "v39", "v40", "v41", "v42", "v43", "v44", "v45", "v46", "v47", "v48", "v49", "v50", "v51", "v52", "v53", "v54", "v55", "v56", "v57", "v58", "v59", "v60", "v61", "v62", "v63", "v64", "v65", "v66", "v67", "v68", "v69", "v70", "v71", "v72", "v73", "v74", "v75", "v76", "v77", "v78", "v79", "v80", "v81", "v82", "v83", "v84", "v85", "v86", "v87", "v88", "v89", "v90", "v91", "v92", "v93", "v94", "v95", "v188", "v189", "v190", "v191", "v192", "v193", "v194", "v195", "v196", "v197", "v198", "v199", "v200", "v201", "v202", "v203", "v204", "v205", "v206", "v207", "v208", "v209", "v210", "v211", "v212", "v213", "v214", "v215", "v228", "v229", "v230", "v231", "v244", "v253"

DI void phaseBC(const BPre& p, char* __restrict__ ws, rsrc_t wsr, bf16_t* __restrict__ out, unsigned tag, int qb, int hg, unsigned wid) {
  const unsigned rg = wid & 3, kh = wid >> 2;
  const int rb = qb >> 2;
  unsigned lane = fresh_lane(), l31 = lane & 31, hh = lane >> 5, lane16 = lane * 16;
  const unsigned ldsh = kh * K2_HALF;
  const float km = wave_max_pos(p.km);
  float cb[2];
  bool unsafe = false;
#pragma unroll
  for (int j = 0; j < 2; ++j) {
    float n2 = 0.f;
#pragma unroll
    for (int i = 0; i < 8; ++i) { float v = (float)p.qa[j][i]; n2 += v * v; }
    if (hh == 0) {
#pragma unroll
      for (int i = 0; i < 4; ++i) { float v = (float)p.qlo[j][i]; n2 += v * v; }
    }
    n2 = half_sum(n2);
    cb[j] = __builtin_amdgcn_sqrtf(n2 * km) * 1.0001f;
    unsafe |= !(cb[j] <= 50.0f);
  }
  if (__any(unsafe)) {
    float mx[2] = {-INFINITY, -INFINITY};
    bf16x4 qz[2];
#pragma unroll
    for (int j = 0; j < 2; ++j) qz[j] = hh ? bf16x4{} : p.qlo[j];
    for (int t = 0; t < 64; ++t) {
      const unsigned kc = WS_KV + (t >> 3) * KV_CHUNK;
      bf16x8 ka = bld128(wsr, lane16, kc + (t & 7) * 1024);
      bf16x4 kb = bld64(wsr, lane * 8, kc + KV_KB + (t & 7) * 512);
#pragma unroll
      for (int j = 0; j < 2; ++j) {
        f32x16 s = mfma32(ka, p.qa[j], f32x16{});
        s = mfma8(kb, qz[j], s);
#pragma unroll
        for (int r = 0; r < 16; ++r) mx[j] = fmaxf(mx[j], s[r]);
      }
    }
#pragma unroll
    for (int j = 0; j < 2; ++j) cb[j] = fmaxf(mx[j], __shfl_xor(mx[j], 32));
  }
  bf16x4 qbv[2];
#pragma unroll
  for (int j = 0; j < 2; ++j) {
    const float c = -cb[j];
    const __bf16 h0 = (__bf16)c; const float r1 = c - (float)h0;
    const __bf16 h1 = (__bf16)r1; const float r2 = r1 - (float)h1;
    const __bf16 h2 = (__bf16)r2;
    qbv[j] = hh ? bf16x4{h0, h1, h2, (__bf16)0.f} : p.qlo[j];
  }
  const unsigned aK = lane16, aKB = KV_KB + lane * 8, aV = KV_V + hh * 336 + l31 * 16;
  const unsigned ru = wid & 1, kq = wid >> 1;
  unsigned fxo = WS_HF + ((qb * 2 + ru) * 108 + kq * 27) * 1024;
  unsigned fwo = WS_WP + ((hg * 148 + kq * 27) * 3) * 1024;
  const unsigned wo0 = WS_WP + (hg * 148 + 108) * 3072;
  WAITV(0); BARRIER();
  f32x16 o0, o1, fa0, fa1, fa2;
  float gq = p.gq, gf = p.gf;
  u32x4 rx0, rx1, rx2, rw0, rw1, rw2;
  {
    unsigned ack = P0 + ldsh + aK, ackb = P0 + ldsh + aKB, acv = P0 + ldsh + aV;
    unsigned ank = RB + ldsh + aK, ankb = RB + ldsh + aKB, anv = RB + ldsh + aV;
    unsigned dlds = RB + ldsh + rg * 1024, dsrc = WS_KV + (kh * 4 + 3) * KV_CHUNK + rg * 1024, dsrc2 = wo0 + (kh * 24 + rg) * 1024;
    const unsigned fi = wid * 64 + lane, hp = (fi - 175) & 7, bp = (hp * 4 + ((qb >> 2) & 3)) * 8 + (qb & 3) * 2 + (qb >> 4);
    unsigned fo0 = fi < 27 ? WS_FH + (rb * 27 + fi) * 128 : (fi < 175 ? WS_FW + (fi - 27) * 128 : (fi < 183 ? WS_FARR + bp * 128 : WS_FH + rb * 27 * 128));
    const unsigned l16b = (lane & 16u) ? 0x7ffffff0u : lane * 16;
    fxo = rfl(fxo); fwo = rfl(fwo); dlds = rfl(dlds); dsrc = rfl(dsrc); dsrc2 = rfl(dsrc2);
    unsigned t0, t1, dlt, cnt; unsigned long long tp;
    rw0 = u32x4{fo0, 0u, 0u, 0u};
    asm volatile(ATT_LOOP_SC1
      : "={v[96:111]}"(o0), "={v[112:127]}"(o1), "={v[128:143]}"(fa0), "={v[144:159]}"(fa1), "={v[160:175]}"(fa2),
        "+{v246}"(ack), "+{v247}"(ackb), "+{v248}"(acv), "+{v249}"(ank), "+{v250}"(ankb), "+{v251}"(anv), "+{v254}"(gq), "+{v255}"(gf),
        "={v[216:219]}"(rx0), "={v[220:223]}"(rx1), "={v[224:227]}"(rx2), "+{v[232:235]}"(rw0), "={v[236:239]}"(rw1), "={v[240:243]}"(rw2),
        [fxo] "+s"(fxo), [fwo] "+s"(fwo), [dlds] "+s"(dlds), [dsrc] "+s"(dsrc), [t0] "=&s"(t0), [t1] "=&s"(t1), [dlt] "=&s"(dlt), [cnt] "=&s"(cnt), [tp] "=&s"(tp)
      : "{v[176:179]}"(p.qa[0]), "{v[180:183]}"(p.qa[1]), "{v[184:185]}"(qbv[0]), "{v[186:187]}"(qbv[1]), "{v245}"(lane16), "{v252}"(l16b),
        [srd] "s"(wsr), [dsrc2] "s"(dsrc2), [tag] "s"(tag)
      : ATT_CLOBBERS, "vcc", "scc", "memory");
  }
  f32x16 fa[3] = {fa0, fa1, fa2};
  lane = fresh_lane(); l31 = lane & 31; hh = lane >> 5; lane16 = lane * 16;
  const unsigned tid = wid * 64 + lane;
  constexpr int NPOST = 27 - ATT_NF_IN;
  static_assert(NPOST >= 3, "");
  const unsigned pw0 = WS_WP + ((hg * 148 + kq * 27 + ATT_NF_IN) * 3) * 1024, px0 = WS_HF + ((qb * 2 + ru) * 108 + kq * 27 + ATT_NF_IN) * 1024;
  const unsigned l16b = (lane & 16u) ? 0x7ffffff0u : lane * 16;
  bf16x8 rw[NPOST - 1][3], rx[NPOST];
  rx[0] = __builtin_bit_cast(bf16x8, rx0); rx[1] = __builtin_bit_cast(bf16x8, rx1); rx[2] = __builtin_bit_cast(bf16x8, rx2);
#pragma unroll
  for (int s = 0; s < NPOST - 1; ++s) {
    rw[s][0] = bld128(wsr, lane16, pw0 + (s * 3 + 3) * 1024); rw[s][1] = bld128(wsr, lane16, pw0 + (s * 3 + 4) * 1024); rw[s][2] = bld128(wsr, l16b, pw0 + (s * 3 + 5) * 1024);
    if (s + 3 < NPOST) rx[s + 3] = bld128(wsr, lane16, px0 + (s + 3) * 1024);
  }
  SB();
  constexpr unsigned WO_LAST = P0 + K2_HALF + KV_V;
  {
    auto slot = [](unsigned s) { return P0 + (s >> 2) * K2_HALF + (s & 3) * 3072; };
    const unsigned xa = slot(rg * 2 + kh) + lane16;
    f32x16 give = kh ? o0 : o1;
#pragma unroll
    for (int g = 0; g < 3; ++g) *(LDSP f32x4*)(size_t)(xa + g * 1024) = f32x4{give[4 * g], give[4 * g + 1], give[4 * g + 2], give[4 * g + 3]};
    BARRIER();
#pragma unroll
    for (int i = 0; i < 3; ++i) dma16b(wsr, lane16, wo0 + (96 + wid * 3 + i) * 1024, WO_LAST + (wid * 3 + i) * 1024);
    const unsigned xb = slot(rg * 2 + (kh ^ 1)) + lane16;
    f32x16 mine = kh ? o1 : o0;
#pragma unroll
    for (int g = 0; g < 3; ++g) { const f32x4 v = *(LDSP const f32x4*)(size_t)(xb + g * 1024); mine[4 * g] += v[0]; mine[4 * g + 1] += v[1]; mine[4 * g + 2] += v[2]; mine[4 * g + 3] += v[3]; }
    auto lsw = __builtin_amdgcn_permlane32_swap(__float_as_uint(mine[8]), __float_as_uint(mine[8]), false, false);
    const float sc = gq * __builtin_amdgcn_rcpf(__uint_as_float(lsw[1]));
    const unsigned rt = qb * 2 + kh;
    u32x4 f0 = {pk2(mine[0] * sc, mine[1] * sc), pk2(mine[2] * sc, mine[3] * sc), pk2(mine[4] * sc, mine[5] * sc), pk2(mine[6] * sc, mine[7] * sc)};
    bst128(f0, wsr, lane16, WS_OF + (rt * 40 + hg * 5 + rg) * 1024);
    if (hh == 0) {
      u32x2 f1 = {pk2(mine[8] * sc, mine[9] * sc), pk2(mine[10] * sc, mine[11] * sc)};
      bst64(f1, wsr, ((rg >> 1) * 32 + l31) * 16 + (rg & 1) * 8, WS_OF + (rt * 40 + hg * 5 + 4) * 1024);
    }
  }
  SB();
  fa[0] = mfma32(__builtin_bit_cast(bf16x8, rw0), rx[0], fa[0]);
  fa[1] = mfma32(__builtin_bit_cast(bf16x8, rw1), rx[0], fa[1]);
  fa[2] = mfma32(__builtin_bit_cast(bf16x8, rw2), rx[0], fa[2]);
#pragma unroll
  for (int s = 0; s < NPOST - 1; ++s)
#pragma unroll
    for (int u = 0; u < 3; ++u) fa[u] = mfma32(rw[s][u], rx[s + 1], fa[u]);
#pragma unroll
  for (int u = 0; u < 3; ++u)
#pragma unroll
    for (int r = 0; r < 16; ++r) fa[u][r] *= (1.0f - gf);
  WAITV(2); BARRIER();
  bf16x8 wf[10][3];
#pragma unroll
  for (int s = 0; s < 10; ++s)
#pragma unroll
    for (int u = 0; u < 3; ++u) { const unsigned pc = kq * 30 + s * 3 + u; wf[s][u] = lds_b128((pc < 96 ? RB + pc * 1024 : WO_LAST + (pc - 96) * 1024) + lane16); }
  BARRIER();
  if (blockIdx.x == 0 && wid == 7) {
    for (unsigned spins = 0; spins < (1u << 20); ++spins) {
      const unsigned f0 = flag_get(ws, WS_FARR + lane * 128), f1 = flag_get(ws, WS_FARR + (64 + lane) * 128);
      const unsigned f2 = flag_get(ws, WS_FARR + (128 + lane) * 128), f3 = flag_get(ws, WS_FARR + (192 + lane) * 128);
      if (__all(f0 == tag && f1 == tag && f2 == tag && f3 == tag)) break;
    }
    if (lane == 0) flag_set(ws, WS_EPOCH, tag);
  }
  {
    u32x4 xf[10] = {};
    const unsigned x0 = WS_OF + ((qb * 2 + ru) * 40 + kq * 10) * 1024;
    const unsigned pl = lane & 15;
    const unsigned pofs = pl < 10 ? pl * 1024 : (pl < 13 ? 4 * 1024 : 9 * 1024) + (((pl - 10) % 3) == 0 ? 8 : (((pl - 10) % 3) == 1 ? 512 : 520));
    for (unsigned spins = 0; spins < 4096u; ++spins) {
      asm volatile("" ::: "memory");
      if (spins) {
        const unsigned pr = __builtin_amdgcn_raw_buffer_load_b32(wsr, pofs, x0, SC1);
        if (__any(pr == ~0u)) continue;
      }
#pragma unroll
      for (int s = 0; s < 10; ++s) xf[s] = __builtin_amdgcn_raw_buffer_load_b128(wsr, lane16, x0 + s * 1024, SC1);
      unsigned mx = 0;
#pragma unroll
      for (int s = 0; s < 10; ++s) { mx = mx > xf[s][0] ? mx : xf[s][0]; mx = mx > xf[s][1] ? mx : xf[s][1]; mx = mx > xf[s][2] ? mx : xf[s][2]; mx = mx > xf[s][3] ? mx : xf[s][3]; }
      if (!__any(mx == ~0u)) break;
    }
#pragma unroll
    for (int s = 0; s < 10; ++s)
#pragma unroll
      for (int u = 0; u < 3; ++u) fa[u] = mfma32(wf[s][u], __builtin_bit_cast(bf16x8, xf[s]), fa[u]);
  }
  {
    constexpr int RS = 100;
    const unsigned wa = ((kq * 64 + ru * 32 + l31) * RS + 4 * hh) * 4;
#pragma unroll
    for (int u = 0; u < 3; ++u)
#pragma unroll
      for (int g = 0; g < 4; ++g) {
        if (u < 2 || g < 2) *(LDSP f32x4*)(size_t)(wa + (u * 32 + g * 8) * 4) = f32x4{fa[u][4 * g], fa[u][4 * g + 1], fa[u][4 * g + 2], fa[u][4 * g + 3]};
      }
    BARRIER();
#pragma unroll
    for (int i = 0; i < 2; ++i) {
      const unsigned it = tid + i * 512;
      if (it < 640) {
        const unsigned row = it / 10, c8 = it - row * 10;
        f32x4 s0 = {}, s1 = {};
#pragma unroll
        for (int k = 0; k < 4; ++k) {
          const unsigned ra = ((k * 64 + row) * RS + c8 * 8) * 4;
          s0 += *(LDSP const f32x4*)(size_t)ra; s1 += *(LDSP const f32x4*)(size_t)(ra + 16);
        }
        u32x4 o = {pk2(s0[0], s0[1]), pk2(s0[2], s0[3]), pk2(s1[0], s1[1]), pk2(s1[2], s1[3])};
        *(u32x4*)(out + (size_t)(qb * 64 + row) * D + hg * 80 + c8 * 8) = o;
      }
    }
  }
}

__global__ __launch_bounds__(512, 2) void mega(const bf16_t* __restrict__ x, const bf16_t* __restrict__ wq, const bf16_t* __restrict__ wk, const bf16_t* __restrict__ wv,
                                               const bf16_t* __restrict__ wg, const bf16_t* __restrict__ wu, const bf16_t* __restrict__ gate_w,
                                               const bf16_t* __restrict__ wd, const bf16_t* __restrict__ wo, char* __restrict__ ws, bf16_t* __restrict__ out) {
  asm volatile("" :: "s"(x), "s"(wg), "s"(wu), "s"(ws));
  const int b = blockIdx.x, xcd = b & 7, idx = b >> 3;
  const unsigned wid = rfl(threadIdx.x >> 6);
  const int grp = xcd >> 1, nffn = grp < 3 ? 28 : 24;
  const bool ffn = idx < nffn;
  const int rb = (xcd & 1) * 4 + (idx & 3), cb = grp * 7 + (idx >> 2);
  const int qb = rb * 4 + grp, hg = idx >> 2;
  const int kvi = hg == 7 ? qb : 32 + (xcd - 6) * 4 + (idx & 3);
  const rsrc_t wsr = make_rsrc(ws, WS_END);
  const unsigned rg = wid & 3, kh = wid >> 2;
  BPre bp; unsigned tag;
  QStage qs;
  f32x16 qacc[2] = {};
  if (__builtin_expect(ffn, 1)) {
    AOut ao; XQ xq;
    const unsigned k0 = KROT(xcd);
    tag = ffn_main(ao, xq, qb, hg, wsr, x, wg + cb * 64, wu + cb * 64, rb, wid, idx >> 2, idx & 3, k0);
    const unsigned lane = fresh_lane();
    const unsigned rt = rb * 8 + (wid >> 1) * 2, ks0 = cb * 4 + (wid & 1) * 2;
    bf16x8 wr[10];
#pragma unroll
    for (int m = 0; m < 6; ++m) wr[m] = xq.w[m];
    {
      const unsigned l31 = lane & 31, wo_ = ((lane >> 5) * 20 + (l31 < 20 ? l31 : 19)) * 16;
#pragma unroll
      for (int m = 6; m < 10; ++m) wr[m] = bld128(wsr, wo_, WS_WQ + ((hg * 80 + 4 * (k0 + 5 + (m >> 1)) + kh + 2 * (m & 1)) * 4 + rg) * 640);
    }
    SB();
    auto xr = [&](int m, int t) { const int o = (m & 1) * 8 + t * 4; const f32x16& v = xq.v[m >> 1]; return __builtin_bit_cast(bf16x8, f32x4{v[o], v[o + 1], v[o + 2], v[o + 3]}); };
    unsigned pk[8];
    {
#pragma unroll
      for (int i = 0; i < 8; ++i) {
        pk[i] = gelu_pair(ao, 0, 2 * i);
        if (i < 5) { qacc[0] = mfma32(wr[2 * i], xr(2 * i, 0), qacc[0]); qacc[1] = mfma32(wr[2 * i], xr(2 * i, 1), qacc[1]);
                     qacc[0] = mfma32(wr[2 * i + 1], xr(2 * i + 1, 0), qacc[0]); qacc[1] = mfma32(wr[2 * i + 1], xr(2 * i + 1, 1), qacc[1]); }
        SB();
      }
      h_store(pk, 0, wsr, lane, rt, ks0);
    }
    if (wid == 0) poll_flags(ws, WS_FKV + ((b & 3) * 32 + (lane & 31)) * 128, lane < 32, tag);
    BARRIER();
    {
      QFrag f; qfrag_read(f, QS_B, lane, rg, kh);
      kv_scalars(bp, wsr, qb, lane, wid);
      SB();
#pragma unroll
      for (int i = 0; i < 5; ++i) {
        pk[i] = gelu_pair(ao, 1, 2 * i);
        qfrag_mma(qacc, f, i);
        SB();
      }
      QFrag f2; qfrag_read(f2, QS_A, lane, rg, kh);
      SB();
#pragma unroll
      for (int i = 5; i < 8; ++i) { pk[i] = gelu_pair(ao, 1, 2 * i); SB(); }
      h_store(pk, 1, wsr, lane, rt, ks0);
#pragma unroll
      for (int i = 0; i < 5; ++i) qfrag_mma(qacc, f2, i);
    }
    BARRIER();
#pragma unroll
    for (int i = 0; i < 6; ++i) kv_piece(wsr, lane, wid, i);
    WAITV(6);
    q_finish<false>(bp, qacc, QS_C, lane, wid);
    { const unsigned tid = wid * 64 + lane;
      if (tid == 0) flag_set(ws, WS_FH + (rb * 27 + cb) * 128, tag);
      if (tid == 1) flag_set(ws, WS_FARR + b * 128, tag); }
  } else {
    tag = kv_main(x, wq, wk, wv, gate_w, wd, wo, kvi, qb, hg, b & 3, bp, wid, ws);
    if (kvi >= 32) {
      const unsigned lane = fresh_lane();
      mark_of(wsr, qb, hg, lane, wid);
      qstage_init(qs, x, qb, hg, lane, wid);
      if (wid == 1) poll_flags(ws, WS_FWQ + (lane < 40 ? lane : 39) * 128, true, tag);
      BARRIER();
      qstage_issue(qs, wsr, 0, QS_C, lane, wid); qstage_issue(qs, wsr, 1, QS_A, lane, wid); qstage_issue(qs, wsr, 2, QS_B, lane, wid);
      WAITV(12); BARRIER();
      qgemm_quarter(qacc, QS_C, lane, rg, kh);
      WAITV(6); BARRIER();
      qstage_issue(qs, wsr, 3, QS_C, lane, wid);
      qgemm_quarter(qacc, QS_A, lane, rg, kh);
      if (wid == 0) poll_flags(ws, WS_FKV + ((b & 3) * 32 + (lane & 31)) * 128, lane < 32, tag);
      WAITV(6); BARRIER();
      qgemm_quarter(qacc, QS_B, lane, rg, kh);
      BARRIER();
#pragma unroll
      for (int i = 0; i < 6; ++i) kv_piece(wsr, lane, wid, i);
      kv_scalars(bp, wsr, qb, lane, wid);
      WAITV(9); BARRIER();
      qgemm_quarter(qacc, QS_C, lane, rg, kh);
      q_finish<true>(bp, qacc, QS_C, lane, wid);
      if (wid == 0 && lane == 1) flag_set(ws, WS_FARR + b * 128, tag);
    }
  }
  tag = rfl(tag);
  phaseBC(bp, ws, wsr, out, tag, qb, hg, wid);
}

extern "C" void kernel_launch(void* const* d_in, const int* in_sizes, int n_in, void* d_out, int out_size, void* d_ws, size_t ws_size, hipStream_t stream) {
  const bf16_t* x = (const bf16_t*)d_in[0];
  const bf16_t* wq = (const bf16_t*)d_in[1];
  const bf16_t* wk = (const bf16_t*)d_in[2];
  const bf16_t* wv = (const bf16_t*)d_in[3];
  const bf16_t* wo = (const bf16_t*)d_in[4];
  const bf16_t* wg = (const bf16_t*)d_in[6];
  const bf16_t* wu = (const bf16_t*)d_in[7];
  const bf16_t* wd = (const bf16_t*)d_in[8];
  const bf16_t* gw = (const bf16_t*)d_in[9];
  char* ws = (char*)d_ws;
  (void)hipFuncSetAttribute((const void*)mega, hipFuncAttributeMaxDynamicSharedMemorySize, MEGA_LDS);
  mega<<<256, 512, MEGA_LDS, stream>>>(x, wq, wk, wv, wg, wu, gw, wd, wo, ws, (bf16_t*)d_out);
}
```
